```python
import torch

def _softmax(x, axis=-1):
    return torch.softmax(x, dim=axis)
def _gelu(x):
    return torch.nn.functional.gelu(x, approximate="tanh")
def _silu(x):
    return torch.nn.functional.silu(x)
def _sigmoid(x):
    return torch.sigmoid(x)
def _layernorm(x, axis=-1, eps=1e-5):
    m = x.mean(dim=axis, keepdim=True)
    v = x.var(dim=axis, keepdim=True, unbiased=False)
    return (x - m) / torch.sqrt(v + eps)
def _scatter_add(tgt, idx, src, axis=0):
    return tgt.clone().index_add_(axis, idx, src)
def _segment_sum(src, seg, n):
    out = torch.zeros((n,) + src.shape[1:], dtype=src.dtype)
    return out.index_add_(0, seg, src)
def _pad(x, pw):
    flat = tuple(v for pair in reversed(pw) for v in pair)
    return torch.nn.functional.pad(x, flat)

kernel_name = 'parallel_sum__gqa_block_cached__dense_activation_gelu__decode'

def setup_inputs():

    _w = (lambda a: (lambda t: t.to(torch.bfloat16) if t.is_floating_point() else t)(torch.as_tensor(a)))
    def _A_setup():
        B, Sq, D = 1, 1, 448
        Hq, Hkv, Sk, Dh = 16, 8, 65536, 256
        assert Hq % Hkv == 0, (Hq, Hkv)
        return {
            "x": _w((lambda *s: torch.randn(*s, dtype=torch.bfloat16) * 0.05)(B, Sq, D)),
            "k_cache": _w((lambda *s: torch.randn(*s, dtype=torch.bfloat16) * 3.2)(B, Hkv, Sk, Dh)),
            "v_cache": _w((lambda *s: torch.randn(*s, dtype=torch.bfloat16) * 1.6)(B, Hkv, Sk, Dh)),
            "wq": _w((lambda *s: torch.randn(*s, dtype=torch.bfloat16) * 3.2)(D, Hq * Dh) * (D ** -0.5)),
            "wo": _w((lambda *s: torch.randn(*s, dtype=torch.bfloat16) * 1.6)(Hq * Dh, D) * ((Hq * Dh) ** -0.5)),
        }
    _A_in = _A_setup()

    def _B_setup():
        B, S, D, F = 1, 1, 448, 1792
        return {
            "x": _w((lambda *s: torch.randn(*s, dtype=torch.bfloat16) * 0.05)(B, S, D)),
            "w1": _w((lambda *s: torch.randn(*s, dtype=torch.bfloat16) * 1.6)(D, F) * (D ** -0.5)),
            "b1": _w((lambda *s: torch.randn(*s, dtype=torch.bfloat16) * 1.6)(F) * 0.01),
            "w2": _w((lambda *s: torch.randn(*s, dtype=torch.bfloat16) * 1.6)(F, D) * (F ** -0.5)),
            "b2": _w((lambda *s: torch.randn(*s, dtype=torch.bfloat16) * 1.6)(D) * 0.01),
        }
    _B_in = _B_setup()

    return {
        "A_x": _A_in["x"],
        "A_k_cache": _A_in["k_cache"],
        "A_v_cache": _A_in["v_cache"],
        "A_wq": _A_in["wq"],
        "A_wo": _A_in["wo"],
        "B_x": _B_in["x"],
        "B_w1": _B_in["w1"],
        "B_b1": _B_in["b1"],
        "B_w2": _B_in["w2"],
        "B_b2": _B_in["b2"],
    }

def reference(A_x, A_k_cache, A_v_cache, A_wq, A_wo, B_x, B_w1, B_b1, B_w2, B_b2):
    def _A_ref(x, k_cache, v_cache, wq, wo):
        B, Sq, D = x.shape
        Hq, Dh = 16, 256
        Hkv = k_cache.shape[1]
        rep = Hq // Hkv
        q = (lambda x, *d: x.permute(*d))((x @ wq).reshape(B, Sq, Hq, Dh), 0, 2, 1, 3)
        k = (lambda x, n, axis: torch.repeat_interleave(x, n, dim=axis))(k_cache, rep, axis=1)
        v = (lambda x, n, axis: torch.repeat_interleave(x, n, dim=axis))(v_cache, rep, axis=1)
        scores = torch.einsum("bhqd,bhkd->bhqk", q, k) * (Dh ** -0.5)
        p = _softmax(scores, axis=-1)
        o = torch.einsum("bhqk,bhkd->bhqd", p, v)
        return (lambda x, *d: x.permute(*d))(o, 0, 2, 1, 3).reshape(B, Sq, Hq * Dh) @ wo

    def _B_ref(x, w1, b1, w2, b2):
        h = torch.einsum("bsd,df->bsf", x, w1) + b1
        h = _gelu(h)
        return torch.einsum("bsf,fd->bsd", h, w2) + b2

    x = A_x
    xn = _layernorm(x, axis=-1)
    return x + _A_ref(xn, A_k_cache, A_v_cache, A_wq, A_wo) + _B_ref(xn, B_w1, B_b1, B_w2, B_b2)
```

```cpp
#include <hip/hip_runtime.h>
#include <hip/hip_bf16.h>
#include <stdint.h>

typedef unsigned u32;
typedef unsigned long long u64;
typedef u32 u32x4 __attribute__((ext_vector_type(4)));
typedef u32 u32x2 __attribute__((ext_vector_type(2)));
typedef float f32x4 __attribute__((ext_vector_type(4)));
typedef __bf16 bf16x8 __attribute__((ext_vector_type(8)));
typedef __bf16 bf16x2 __attribute__((ext_vector_type(2)));
typedef short s16x4 __attribute__((ext_vector_type(4)));
#define DI __device__ __forceinline__

#define MIMIC_LN 1
#define MIMIC_S 1
#define MIMIC_O 1
#define DEPTH 2
#define STAMPS 0

constexpr int D = 448, HQ = 16, HKV = 8, SK = 65536, DH = 256, FF = 1792;
constexpr int NWG = 256, NSPLIT = 32, NWAVE = 4;
constexpr int NT = SK / NSPLIT / NWAVE / 16;
constexpr int NFIN = 28;
constexpr u32 WS_EPOCH = 0;
constexpr u32 WS_QG = 4096;
constexpr u32 WS_ML = WS_QG + 8 * 512 * 8;
constexpr u32 WS_OA = WS_ML + 8 * 32 * 16;
constexpr u32 WS_YC = WS_OA + 8 * 32 * 32 * 64;
constexpr u32 WS_END = WS_YC + NFIN * NWG * 64;
constexpr u32 WS_STAMP = 2u << 20;
constexpr u32 SENT = 0xFFFFFFFFu;
constexpr int AUX_NT = 2, AUX_SC1 = 16;
constexpr u32 SPIN_CAP = 1u << 15;

#define MEMFENCE() asm volatile("" ::: "memory")
#define SCHED() __builtin_amdgcn_sched_barrier(0)

DI float bf2f_lo(u32 x) { return __builtin_bit_cast(float, x << 16); }
DI float bf2f_hi(u32 x) { return __builtin_bit_cast(float, x & 0xffff0000u); }
DI u32 pk_bf16(float a, float b) { bf16x2 v = {(__bf16)a, (__bf16)b}; return __builtin_bit_cast(u32, v); }
DI float bfr(float a) { return bf2f_lo(pk_bf16(a, 0.f)); }
template <int CTRL> DI float dpp(float x) {
  return __builtin_bit_cast(float, __builtin_amdgcn_mov_dpp(__builtin_bit_cast(int, x), CTRL, 0xf, 0xf, true));
}
constexpr int XOR1 = 0xB1, XOR2 = 0x4E, XOR7 = 0x141, XOR8 = 0x128, ROR4 = 0x124;
DI float x16_sum(float x) { auto s = __builtin_amdgcn_permlane16_swap(__float_as_uint(x), __float_as_uint(x), false, false); return __uint_as_float(s[0]) + __uint_as_float(s[1]); }
DI float x32_sum(float x) { auto s = __builtin_amdgcn_permlane32_swap(__float_as_uint(x), __float_as_uint(x), false, false); return __uint_as_float(s[0]) + __uint_as_float(s[1]); }
DI float x16_max(float x) { auto s = __builtin_amdgcn_permlane16_swap(__float_as_uint(x), __float_as_uint(x), false, false); return fmaxf(__uint_as_float(s[0]), __uint_as_float(s[1])); }
DI float x32_max(float x) { auto s = __builtin_amdgcn_permlane32_swap(__float_as_uint(x), __float_as_uint(x), false, false); return fmaxf(__uint_as_float(s[0]), __uint_as_float(s[1])); }
DI float row_sum(float x) { x += dpp<XOR1>(x); x += dpp<XOR2>(x); x += dpp<XOR7>(x); x += dpp<XOR8>(x); return x; }
DI float row_max(float x) { x = fmaxf(x, dpp<XOR1>(x)); x = fmaxf(x, dpp<XOR2>(x)); x = fmaxf(x, dpp<XOR7>(x)); x = fmaxf(x, dpp<XOR8>(x)); return x; }
DI float wave_sum(float x) { return x32_sum(x16_sum(row_sum(x))); }
DI u32 san(float v) { u32 b = __float_as_uint(v); return b == SENT ? 0x7FFFFFFFu : b; }
DI bool valid4(u32x4 v) { return v[0] != SENT && v[1] != SENT && v[2] != SENT && v[3] != SENT; }
DI void bar() { asm volatile("s_waitcnt lgkmcnt(0)\n\ts_barrier" ::: "memory"); }

struct Params {
  const void* x; const void* k; const void* v; const void* wq; const void* wo;
  const void* w1; const void* b1; const void* w2; const void* b2;
  void* out; void* ws;
};

constexpr int L_XN = 0;
constexpr int L_QP = L_XN + 2048;
constexpr int L_HP = L_QP + 256;
constexpr int L_Q = L_HP + 128;
constexpr int QSTR = 576;
constexpr int L_WO = L_Q + 2 * QSTR;
constexpr int L_W2 = L_WO + 16 * 896;
constexpr int L_RED = L_W2 + 8 * 896;
constexpr int L_ML = L_RED + 8192;
constexpr int L_O = L_ML + 64;
constexpr int L_FIN = L_O + 64;
constexpr int L_END = L_FIN + 256;

__global__ __launch_bounds__(256, 2) void fused_decode(Params p) {
  __shared__ __attribute__((aligned(16))) char lds[L_END];
  const int tid = threadIdx.x, lane = tid & 63;
  const int w = __builtin_amdgcn_readfirstlane(tid >> 6);
  const int bid = blockIdx.x, g = bid & 7, s = bid >> 3;
  const int lg = lane >> 4, lc = lane & 15;
#define STAMP(i)
  STAMP(0);
  const auto rK = __builtin_amdgcn_make_buffer_rsrc((void*)p.k, 0, 0x10000000, 0x00020000);
  const auto rV = __builtin_amdgcn_make_buffer_rsrc((void*)p.v, 0, 0x10000000, 0x00020000);
  const auto rW = __builtin_amdgcn_make_buffer_rsrc(p.ws, 0, WS_END, 0x00020000);
  const auto rX = __builtin_amdgcn_make_buffer_rsrc((void*)p.x, 0, D * 2, 0x00020000);
  const auto rWq = __builtin_amdgcn_make_buffer_rsrc((void*)p.wq, 0, D * HQ * DH * 2, 0x00020000);
  const auto rWo = __builtin_amdgcn_make_buffer_rsrc((void*)p.wo, 0, D * HQ * DH * 2, 0x00020000);
  const auto rW1 = __builtin_amdgcn_make_buffer_rsrc((void*)p.w1, 0, D * FF * 2, 0x00020000);
  const auto rW2 = __builtin_amdgcn_make_buffer_rsrc((void*)p.w2, 0, D * FF * 2, 0x00020000);
  const auto rB1 = __builtin_amdgcn_make_buffer_rsrc((void*)p.b1, 0, FF * 2, 0x00020000);

  const u32x2 ep = __builtin_amdgcn_raw_buffer_load_b64(rW, WS_EPOCH, 0, AUX_SC1);
  {
    u32 off;
    if (tid < 128) off = WS_OA + ((g * 32 + (tid >> 2)) * 32 + s) * 64 + (tid & 3) * 16;
    else if (tid < 240) off = WS_YC + (((tid - 128) >> 2) * NWG + bid) * 64 + (tid & 3) * 16;
    else off = WS_ML + (g * 32 + s) * 16;
    const u32x4 sv = {SENT, SENT, SENT, SENT};
    if (tid <= 240) __builtin_amdgcn_raw_buffer_store_b128(sv, rW, off, 0, AUX_SC1);
  }
  MEMFENCE();
  const u32x4 xr = __builtin_amdgcn_raw_buffer_load_b128(rX, lane * 16, 0, 0);
  u32x4 wq[4];
#pragma unroll
  for (int i = 0; i < 4; ++i)
    wq[i] = __builtin_amdgcn_raw_buffer_load_b128(rWq, (32 * (w + 4 * i) + (lane >> 1)) * 8192 + (g * 512 + s * 16) * 2 + (lane & 1) * 16, 0, 0);
  const int lcol = g + 8 * (s >> 3);
  const bool has_mlp = lcol < 28;
  const int u = (has_mlp ? lcol : 0) * 8 + (s & 7);
  u32x4 w1r[2];
#pragma unroll
  for (int i = 0; i < 2; ++i) w1r[i] = __builtin_amdgcn_raw_buffer_load_b128(rW1, (64 * (w + 4 * i) + lane) * (FF * 2) + u * 16, 0, 0);
  const u32 b1r = __builtin_amdgcn_raw_buffer_load_b16(rB1, (u * 8 + (lane & 7)) * 2, 0, 0);
  u32x4 wor[4], w2r[2];
  const u32 rowoff = lane < 56 ? lane * 16 : 0x7fffff00u;
#pragma unroll
  for (int i = 0; i < 4; ++i) wor[i] = __builtin_amdgcn_raw_buffer_load_b128(rWo, rowoff + (g * 512 + s * 16 + w * 4 + i) * 896, 0, AUX_NT);
#pragma unroll
  for (int i = 0; i < 2; ++i) w2r[i] = __builtin_amdgcn_raw_buffer_load_b128(rW2, rowoff + (u * 8 + w * 2 + i) * 896, 0, AUX_NT);
  MEMFENCE();

  u32x4 kb[DEPTH][8], vb[DEPTH][8];
  const u32 koff = (4 * (lc & 3) + (lc >> 2)) * 512 + lg * 16;
  const u32 voff = lg * 512 + lc * 16;
  const u32 tbase = g * (u32)(SK * 512) + (s * (SK / NSPLIT) + w * 16) * 512u;
  constexpr u32 TSTR = NWAVE * 16 * 512;
  auto loadK = [&](int d, int t) {
    u32 o = koff; asm volatile("" : "+v"(o));
#pragma unroll
    for (int j = 0; j < 8; ++j) kb[d][j] = __builtin_amdgcn_raw_buffer_load_b128(rK, o + j * 64, tbase + t * TSTR, AUX_NT);
    MEMFENCE(); SCHED();
  };
  auto loadV = [&](int d, int t) {
    u32 o = voff; asm volatile("" : "+v"(o));
#pragma unroll
    for (int j = 0; j < 8; ++j) vb[d][j] = __builtin_amdgcn_raw_buffer_load_b128(rV, o + ((j >> 1) & 1) * 2048 + (j & 1) * 256, tbase + t * TSTR + (j >> 2) * 4096, AUX_NT);
    MEMFENCE(); SCHED();
  };
  loadK(0, 0); loadV(0, 0);

  float* xn = (float*)(lds + L_XN);
  {
    float xv[8];
#pragma unroll
    for (int e = 0; e < 4; ++e) { xv[2 * e] = bf2f_lo(xr[e]); xv[2 * e + 1] = bf2f_hi(xr[e]); }
    float sum = 0.f;
#pragma unroll
    for (int e = 0; e < 8; ++e) sum += xv[e];
    const float mean = wave_sum(sum) * (1.f / D);
    float sq = 0.f;
#pragma unroll
    for (int e = 0; e < 8; ++e) { const float d = xv[e] - mean; sq = fmaf(d, d, sq); }
    sq = lane < 56 ? sq : 0.f;
    const float var = wave_sum(sq) * (1.f / D);
    f32x4 o[2];
    const float mb = bfr(mean), den = bfr(sqrtf(bfr(bfr(var) + 1e-5f)));
#pragma unroll
    for (int e = 0; e < 8; ++e) o[e >> 2][e & 3] = lane < 56 ? bfr(bfr(xv[e] - mb) / den) : 0.f;
    *(f32x4*)(xn + lane * 8) = o[0]; *(f32x4*)(xn + lane * 8 + 4) = o[1];
  }
  {
    float a[8], h[8];
#pragma unroll
    for (int e = 0; e < 8; ++e) { a[e] = 0.f; h[e] = 0.f; }
#pragma unroll
    for (int i = 0; i < 4; ++i) { const float xx = xn[32 * (w + 4 * i) + (lane >> 1)];
#pragma unroll
      for (int e = 0; e < 4; ++e) { a[2 * e] = fmaf(xx, bf2f_lo(wq[i][e]), a[2 * e]); a[2 * e + 1] = fmaf(xx, bf2f_hi(wq[i][e]), a[2 * e + 1]); } }
#pragma unroll
    for (int e = 0; e < 8; ++e) { float t = a[e]; t += dpp<XOR2>(t); t += dpp<ROR4>(t); t += dpp<XOR8>(t); a[e] = x32_sum(x16_sum(t)); }
    if (lane < 2) { float* qp = (float*)(lds + L_QP) + w * 16 + lane * 8;
      *(f32x4*)qp = f32x4{a[0], a[1], a[2], a[3]}; *(f32x4*)(qp + 4) = f32x4{a[4], a[5], a[6], a[7]}; }
#pragma unroll
    for (int i = 0; i < 2; ++i) { const float xx = xn[64 * (w + 4 * i) + lane];
#pragma unroll
      for (int e = 0; e < 4; ++e) { h[2 * e] = fmaf(xx, bf2f_lo(w1r[i][e]), h[2 * e]); h[2 * e + 1] = fmaf(xx, bf2f_hi(w1r[i][e]), h[2 * e + 1]); } }
#pragma unroll
    for (int e = 0; e < 8; ++e) h[e] = wave_sum(h[e]);
    if (lane == 0) { float* hp = (float*)(lds + L_HP) + w * 8;
      *(f32x4*)hp = f32x4{h[0], h[1], h[2], h[3]}; *(f32x4*)(hp + 4) = f32x4{h[4], h[5], h[6], h[7]}; }
  }
  bar();
  STAMP(1);
  const u64 ep64 = ((u64)(u32)__builtin_amdgcn_readfirstlane(ep[1]) << 32) | (u32)__builtin_amdgcn_readfirstlane(ep[0]);
  const u64 epn = ep64 * 6364136223846793005ull + 1442695040888963407ull;
  const u32 tag_lo = (u32)epn << 16, tag_hi = (u32)(epn >> 16);
  if (w == 0 && lane < 16) {
    const float* qp = (const float*)(lds + L_QP) + lane;
    const float q = (qp[0] + qp[16]) + (qp[32] + qp[48]);
    const u32x2 gr = {tag_lo | (pk_bf16(q, 0.f) & 0xffffu), tag_hi};
    __builtin_amdgcn_raw_buffer_store_b64(gr, rW, WS_QG + (g * 512 + s * 16 + lane) * 8, 0, AUX_SC1);
  }
  MEMFENCE();
  if (lane < 56) {
#pragma unroll
    for (int i = 0; i < 4; ++i) *(u32x4*)(lds + L_WO + (w * 4 + i) * 896 + lane * 16) = wor[i];
#pragma unroll
    for (int i = 0; i < 2; ++i) *(u32x4*)(lds + L_W2 + (w * 2 + i) * 896 + lane * 16) = w2r[i];
  }
  const u32 qgoff = WS_QG + (g * 512 + w * 128 + lane * 2) * 8;
  MEMFENCE(); SCHED();
  u32x4 q1 = __builtin_amdgcn_raw_buffer_load_b128(rW, qgoff, 0, AUX_SC1);
  MEMFENCE(); SCHED();
  loadK(1, 1);
  u32x4 q2 = __builtin_amdgcn_raw_buffer_load_b128(rW, qgoff, 0, AUX_SC1);
  MEMFENCE(); SCHED();
  loadV(1, 1);
  auto qok = [&](u32x4 q) { return (q[0] & 0xffff0000u) == tag_lo && q[1] == tag_hi && (q[2] & 0xffff0000u) == tag_lo && q[3] == tag_hi; };
  if (!__all(qok(q1))) {
    q1 = q2;
    for (u32 spins = 0; !__all(qok(q1)) && spins < SPIN_CAP; ++spins) {
      MEMFENCE();
      q1 = __builtin_amdgcn_raw_buffer_load_b128(rW, qgoff, 0, AUX_SC1);
      asm volatile("" : "+v"(q1));
    }
  }
  *(u32*)(lds + L_Q + (w >> 1) * QSTR + (w & 1) * 256 + lane * 4) = (q1[0] & 0xffffu) | (q1[2] << 16);
  bar();
  STAMP(2);
  const char* qfp = lds + L_Q + (lc & 1) * QSTR + lg * 16;

  constexpr float C2 = 0.0625f * 1.4426950408889634f;
  float m = -INFINITY, l = 0.f;
  f32x4 acc[16];
#pragma unroll
  for (int i = 0; i < 16; ++i) acc[i] = f32x4{0.f, 0.f, 0.f, 0.f};
  auto step = [&](int d, int tn, bool more) {
    f32x4 sc = {0.f, 0.f, 0.f, 0.f};
#pragma unroll
    for (int j = 0; j < 8; ++j) sc = __builtin_amdgcn_mfma_f32_16x16x32_bf16(__builtin_bit_cast(bf16x8, kb[d][j]), *(const bf16x8*)(qfp + j * 64), sc, 0, 0, 0);
    asm volatile("" : "+v"(sc) :: "memory"); SCHED();
    if (more) loadK(d, tn);
    float t[4];
    { const u32 a = pk_bf16(sc[0], sc[1]), b = pk_bf16(sc[2], sc[3]);
      t[0] = bf2f_lo(a) * C2; t[1] = bf2f_hi(a) * C2; t[2] = bf2f_lo(b) * C2; t[3] = bf2f_hi(b) * C2; }
    const float tm = x32_max(x16_max(fmaxf(fmaxf(t[0], t[1]), fmaxf(t[2], t[3]))));
    if (__any(tm > m)) {
      const float mn = fmaxf(m, tm), al = __builtin_amdgcn_exp2f(m - mn);
      m = mn; l *= al;
      const float a0 = dpp<0xA0>(al), a1 = dpp<0xF5>(al);
#pragma unroll
      for (int i = 0; i < 16; ++i) { acc[i][0] *= a0; acc[i][1] *= a1; }
    }
    const float p0 = __builtin_amdgcn_exp2f(t[0] - m), p1 = __builtin_amdgcn_exp2f(t[1] - m), p2 = __builtin_amdgcn_exp2f(t[2] - m), p3 = __builtin_amdgcn_exp2f(t[3] - m);
    l += (p0 + p1) + (p2 + p3);
    const u32x2 pp = {pk_bf16(p0, p1), pk_bf16(p2, p3)};
    const s16x4 pf = __builtin_bit_cast(s16x4, pp);
#pragma unroll
    for (int hf = 0; hf < 2; ++hf)
#pragma unroll
      for (int e = 0; e < 8; ++e) {
        const u32 sel = (e & 1) ? 0x07060302u : 0x05040100u;
        const u32x2 bb = {__builtin_amdgcn_perm(vb[d][2 + hf][e >> 1], vb[d][hf][e >> 1], sel), __builtin_amdgcn_perm(vb[d][6 + hf][e >> 1], vb[d][4 + hf][e >> 1], sel)};
        acc[hf * 8 + e] = __builtin_amdgcn_mfma_f32_16x16x16bf16_1k(pf, __builtin_bit_cast(s16x4, bb), acc[hf * 8 + e], 0, 0, 0);
      }
    MEMFENCE(); SCHED();
    if (more) loadV(d, tn);
  };
  constexpr int NMAIN = (NT - DEPTH) / DEPTH * DEPTH;
#pragma unroll
  for (int d = 0; d < DEPTH; ++d) step(d, d + DEPTH, true);
  asm volatile("" :: "v"(q2));
  for (int t0 = DEPTH; t0 < NMAIN; t0 += DEPTH) {
#pragma unroll
    for (int d = 0; d < DEPTH; ++d) step(d, t0 + d + DEPTH, true);
  }
#pragma unroll
  for (int t = NMAIN; t < NT; ++t) step(t % DEPTH, t + DEPTH, t + DEPTH < NT);
  STAMP(3);

  l = x32_sum(x16_sum(l));
  {
    float* red = (float*)(lds + L_RED) + w * 512 + (lg & 1) * 256 + (lg >> 1) * 128 + lc * 8;
    float o[8];
#pragma unroll
    for (int e = 0; e < 8; ++e) { const float lo = (lg & 1) ? acc[e][1] : acc[e][0], hi = (lg & 1) ? acc[8 + e][1] : acc[8 + e][0]; o[e] = (lg >> 1) ? hi : lo; }
    *(f32x4*)red = f32x4{o[0], o[1], o[2], o[3]}; *(f32x4*)(red + 4) = f32x4{o[4], o[5], o[6], o[7]};
    if (lane < 2) *(float2*)((float*)(lds + L_ML) + w * 4 + lane * 2) = make_float2(m, l);
  }
  bar();
  if (w < 3) {
    const int hh = w & 1;
    const float* ml = (const float*)(lds + L_ML);
    if (w < 2) {
      const float m0 = ml[hh * 2], m1 = ml[4 + hh * 2], m2 = ml[8 + hh * 2], m3 = ml[12 + hh * 2];
      const float M = fmaxf(fmaxf(m0, m1), fmaxf(m2, m3));
      const float e0 = __builtin_amdgcn_exp2f(m0 - M), e1 = __builtin_amdgcn_exp2f(m1 - M), e2 = __builtin_amdgcn_exp2f(m2 - M), e3 = __builtin_amdgcn_exp2f(m3 - M);
      const float* red = (const float*)(lds + L_RED) + hh * 256 + lane * 4;
      const f32x4 o = (*(const f32x4*)red * e0 + *(const f32x4*)(red + 512) * e1) + (*(const f32x4*)(red + 1024) * e2 + *(const f32x4*)(red + 1536) * e3);
      const u32x4 ov = {san(o[0]), san(o[1]), san(o[2]), san(o[3])};
      __builtin_amdgcn_raw_buffer_store_b128(ov, rW, WS_OA + ((g * 32 + hh * 16 + (lane >> 2)) * 32 + s) * 64 + (lane & 3) * 16, 0, AUX_SC1);
    } else if (lane == 0) {
      u32x4 ov;
#pragma unroll
      for (int h2 = 0; h2 < 2; ++h2) {
        const float m0 = ml[h2 * 2], m1 = ml[4 + h2 * 2], m2 = ml[8 + h2 * 2], m3 = ml[12 + h2 * 2];
        const float M = fmaxf(fmaxf(m0, m1), fmaxf(m2, m3));
        const float L = (ml[h2 * 2 + 1] * __builtin_amdgcn_exp2f(m0 - M) + ml[4 + h2 * 2 + 1] * __builtin_amdgcn_exp2f(m1 - M)) +
                        (ml[8 + h2 * 2 + 1] * __builtin_amdgcn_exp2f(m2 - M) + ml[12 + h2 * 2 + 1] * __builtin_amdgcn_exp2f(m3 - M));
        ov[h2 * 2] = san(M); ov[h2 * 2 + 1] = san(L);
      }
      __builtin_amdgcn_raw_buffer_store_b128(ov, rW, WS_ML + (g * 32 + s) * 16, 0, AUX_SC1);
    }
  }
  MEMFENCE();
  STAMP(4);
  float y0 = 0.f, y1 = 0.f;
  {
    const float* hp = (const float*)(lds + L_HP) + (lane & 7);
    float hv = (hp[0] + hp[8]) + (hp[16] + hp[24]);
    hv = bfr(bfr(hv) + bf2f_lo(b1r));
    hv = 0.5f * hv * (1.f + tanhf(0.7978845608028654f * (hv + 0.044715f * hv * hv * hv)));
    hv = has_mlp ? bfr(hv) : 0.f;
    const u32* w2l = (const u32*)(lds + L_W2) + w * 56 + (lane < 56 ? lane : 0);
#pragma unroll
    for (int r = 0; r < 8; ++r) { const float hr = __builtin_bit_cast(float, __builtin_amdgcn_readlane(__builtin_bit_cast(int, hv), r)); const u32 ww = w2l[r * 224];
      y0 = fmaf(hr, bf2f_lo(ww), y0); y1 = fmaf(hr, bf2f_hi(ww), y1); }
  }
  if (bid < NFIN) {
    const u32 off = WS_QG + ((tid & 7) * 512 + (tid >> 3) * 16) * 8;
    for (u32 spins = 0; spins < SPIN_CAP; ++spins) {
      MEMFENCE();
      const u32x2 t = __builtin_amdgcn_raw_buffer_load_b64(rW, off, 0, AUX_SC1);
      if (__all((t[0] & 0xffff0000u) == tag_lo && t[1] == tag_hi)) break;
    }
  }
  {
    const int sp = lane & 31, hh = s >> 4;
    const u32 ooff = WS_OA + ((g * 32 + s) * 32 + sp) * 64 + w * 16, moff = WS_ML + (g * 32 + sp) * 16 + hh * 8;
    u32x4 ov = {SENT, SENT, SENT, SENT}; u32x2 mv = {SENT, SENT};
    bool ok = false;
    for (u32 spins = 0; spins < SPIN_CAP; ++spins) {
      MEMFENCE();
      if (!ok) {
        ov = __builtin_amdgcn_raw_buffer_load_b128(rW, ooff, 0, AUX_SC1);
        mv = __builtin_amdgcn_raw_buffer_load_b64(rW, moff, 0, AUX_SC1);
        ok = valid4(ov) && mv[0] != SENT && mv[1] != SENT;
      }
      if (__all(ok)) break;
    }
    const float ms = __uint_as_float(mv[0]), ls = __uint_as_float(mv[1]);
    const float M = x16_max(row_max(ms));
    const float wt = __builtin_amdgcn_exp2f(ms - M);
    const float L = x16_sum(row_sum(wt * ls));
    f32x4 o;
#pragma unroll
    for (int e = 0; e < 4; ++e) { float t = x16_sum(row_sum(wt * __uint_as_float(ov[e]))) / L;
      t = bfr(t);
      o[e] = t; }
    if (lane == 0) *(f32x4*)((float*)(lds + L_O) + w * 4) = o;
  }
  bar();
  STAMP(5);
  {
    const u32* wol = (const u32*)(lds + L_WO) + w * 56 + (lane < 56 ? lane : 0);
    const float* ol = (const float*)(lds + L_O);
#pragma unroll
    for (int r = 0; r < 16; ++r) { const float orr = ol[r]; const u32 ww = wol[r * 224];
      y0 = fmaf(orr, bf2f_lo(ww), y0); y1 = fmaf(orr, bf2f_hi(ww), y1); }
    const float z0 = dpp<XOR1>(y0), z1 = dpp<XOR1>(y1);
    if (lane < 56 && !(lane & 1)) {
      const int col = w * 112 + lane * 2;
      const u32x4 ov = {san(y0), san(y1), san(z0), san(z1)};
      __builtin_amdgcn_raw_buffer_store_b128(ov, rW, WS_YC + ((col >> 4) * NWG + bid) * 64 + (col & 15) * 4, 0, AUX_SC1);
    }
  }
  MEMFENCE();
  STAMP(6);
  if (bid < NFIN) {
    const u32 off = WS_YC + (bid * NWG + tid) * 64;
    u32x4 yv[4];
    bool ok = false;
    for (u32 spins = 0; spins < SPIN_CAP; ++spins) {
      MEMFENCE();
      if (!ok) {
#pragma unroll
        for (int i = 0; i < 4; ++i) yv[i] = __builtin_amdgcn_raw_buffer_load_b128(rW, off + i * 16, 0, AUX_SC1);
        ok = valid4(yv[0]) && valid4(yv[1]) && valid4(yv[2]) && valid4(yv[3]);
      }
      if (__all(ok)) break;
    }
    float v[16];
#pragma unroll
    for (int i = 0; i < 16; ++i) v[i] = __uint_as_float(yv[i >> 2][i & 3]);
#define TR_STEP(HALF, CTRL, BIT) _Pragma("unroll") for (int h = 0; h < (HALF); ++h) { \
      const float t1 = v[h] + dpp<CTRL>(v[h]), t2 = v[h + (HALF)] + dpp<CTRL>(v[h + (HALF)]); v[h] = (lc & (BIT)) ? t2 : t1; }
    TR_STEP(8, XOR8, 8) TR_STEP(4, XOR7, 4) TR_STEP(2, XOR2, 2) TR_STEP(1, XOR1, 1)
#undef TR_STEP
    const float tot = x32_sum(x16_sum(v[0]));
    if (lane < 16) ((float*)(lds + L_FIN))[w * 16 + lane] = tot;
    bar();
    if (tid < 16) {
      const float* fin = (const float*)(lds + L_FIN) + tid;
      const int col = bid * 16 + tid;
      const float xv = __bfloat162float(((const __hip_bfloat16*)p.x)[col]), bv = __bfloat162float(((const __hip_bfloat16*)p.b2)[col]);
      const float r = xv + ((fin[0] + fin[16]) + (fin[32] + fin[48])) + bv;
      ((unsigned short*)p.out)[col] = (unsigned short)(pk_bf16(r, 0.f) & 0xffffu);
      if (bid == 0 && tid == 0) { const u32x2 e2 = {(u32)epn, (u32)(epn >> 32)}; __builtin_amdgcn_raw_buffer_store_b64(e2, rW, WS_EPOCH, 0, AUX_SC1); }
    }
  }
}

extern "C" void kernel_launch(void* const* d_in, const int* in_sizes, int n_in, void* d_out, int out_size, void* d_ws, size_t ws_size, hipStream_t stream) {
  Params p;
  p.x = d_in[0]; p.k = d_in[1]; p.v = d_in[2]; p.wq = d_in[3]; p.wo = d_in[4];
  p.w1 = d_in[6]; p.b1 = d_in[7]; p.w2 = d_in[8]; p.b2 = d_in[9];
  p.out = d_out; p.ws = d_ws;
  hipLaunchKernelGGL(fused_decode, dim3(NWG), dim3(256), 0, stream, p);
}
```

```cpp
#include <hip/hip_runtime.h>
#include <hip/hip_bf16.h>
#include <stdint.h>

typedef unsigned u32;
typedef unsigned long long u64;
typedef u32 u32x4 __attribute__((ext_vector_type(4)));
typedef u32 u32x2 __attribute__((ext_vector_type(2)));
typedef float f32x4 __attribute__((ext_vector_type(4)));
typedef __bf16 bf16x8 __attribute__((ext_vector_type(8)));
typedef __bf16 bf16x2 __attribute__((ext_vector_type(2)));
typedef short s16x4 __attribute__((ext_vector_type(4)));
#define DI __device__ __forceinline__

#define STAMPS 0
#define LOGB 4
#define QDELAY 0
#define QGAP 0
#define STAGGER 0
#define W1AUX 2
#define WQAUX 2
#define ABL 0
#define TSLEEP 48
#define ROT 0
#define VAR 0
#define KPAT 5
#define NPOLL 8
#define PSLEEP 2
#define OPT 1
#define HPRIO 3

constexpr int D = 448, HQ = 16, HKV = 8, SK = 65536, DH = 256, FF = 1792;
constexpr int NWG = 256, NSPLIT = 32, NSW = 8, NWAVE_TOTAL = 8;
constexpr int NTW = SK / NSPLIT / 16;
constexpr int NFIN = 28;
constexpr u32 WS_EPOCH = 0;
constexpr u32 WS_QG = 4096;
constexpr u32 WS_OA = WS_QG + 8 * 512 * 8;
constexpr u32 WS_YC = WS_OA + 8 * 32 * 32 * 128;
constexpr u32 WS_END = WS_YC + NFIN * NWG * 64;
constexpr u32 WS_STAMP = 2u << 20;
static_assert(WS_END <= WS_STAMP, "");
constexpr u32 SENT = 0xFFFFFFFFu;
constexpr int AUX_NT = 2, AUX_SC1 = 16;
constexpr u32 SPIN_CAP = 1u << 15;
constexpr u32 OOB = 0x7fffff00u;

#define MEMFENCE() asm volatile("" ::: "memory")
#define SCHED() __builtin_amdgcn_sched_barrier(0)

DI float bf2f_lo(u32 x) { return __builtin_bit_cast(float, x << 16); }
DI float bf2f_hi(u32 x) { return __builtin_bit_cast(float, x & 0xffff0000u); }
DI u32 pk_bf16(float a, float b) { bf16x2 v = {(__bf16)a, (__bf16)b}; return __builtin_bit_cast(u32, v); }
DI float bfr(float a) { return bf2f_lo(pk_bf16(a, 0.f)); }
template <int CTRL> DI float dpp(float x) {
  return __builtin_bit_cast(float, __builtin_amdgcn_mov_dpp(__builtin_bit_cast(int, x), CTRL, 0xf, 0xf, true));
}
constexpr int XOR1 = 0xB1, XOR2 = 0x4E, XOR3 = 0x1B, XOR7 = 0x141, XOR8 = 0x128, ROR4 = 0x124;
DI float x16_sum(float x) { auto s = __builtin_amdgcn_permlane16_swap(__float_as_uint(x), __float_as_uint(x), false, false); return __uint_as_float(s[0]) + __uint_as_float(s[1]); }
DI float x32_sum(float x) { auto s = __builtin_amdgcn_permlane32_swap(__float_as_uint(x), __float_as_uint(x), false, false); return __uint_as_float(s[0]) + __uint_as_float(s[1]); }
DI float x16_max(float x) { auto s = __builtin_amdgcn_permlane16_swap(__float_as_uint(x), __float_as_uint(x), false, false); return fmaxf(__uint_as_float(s[0]), __uint_as_float(s[1])); }
DI float x32_max(float x) { auto s = __builtin_amdgcn_permlane32_swap(__float_as_uint(x), __float_as_uint(x), false, false); return fmaxf(__uint_as_float(s[0]), __uint_as_float(s[1])); }
DI float t32_sum(float u, float v) { auto s = __builtin_amdgcn_permlane32_swap(__float_as_uint(u), __float_as_uint(v), false, false); return __uint_as_float(s[0]) + __uint_as_float(s[1]); }
DI float t16_sum(float u, float v) { auto s = __builtin_amdgcn_permlane16_swap(__float_as_uint(u), __float_as_uint(v), false, false); return __uint_as_float(s[0]) + __uint_as_float(s[1]); }
DI float row_sum(float x) { x += dpp<XOR1>(x); x += dpp<XOR2>(x); x += dpp<XOR7>(x); x += dpp<XOR8>(x); return x; }
DI float row_max(float x) { x = fmaxf(x, dpp<XOR1>(x)); x = fmaxf(x, dpp<XOR2>(x)); x = fmaxf(x, dpp<XOR7>(x)); x = fmaxf(x, dpp<XOR8>(x)); return x; }
DI float wave_sum(float x) { return x32_sum(x16_sum(row_sum(x))); }
DI u32 san(float v) { u32 b = __float_as_uint(v); return b == SENT ? 0x7FFFFFFFu : b; }
DI bool valid4(u32x4 v) { return v[0] != SENT && v[1] != SENT && v[2] != SENT && v[3] != SENT; }
DI void bar() { asm volatile("s_waitcnt lgkmcnt(0)\n\ts_barrier" ::: "memory"); }
DI float ex2(float x) { return __builtin_amdgcn_exp2f(x); }

struct Params {
  const void* x; const void* k; const void* v; const void* wq; const void* wo;
  const void* w1; const void* b1; const void* w2; const void* b2;
  void* out; void* ws;
};

constexpr int L_XN = 0;
constexpr int QSTR = 576;
constexpr int L_Q = L_XN + 2048;
constexpr int L_WO = L_Q + 2 * QSTR;
constexpr int L_W2 = L_WO + 16 * 896;
constexpr int L_QP = L_W2 + 8 * 896;
constexpr int L_HP = L_QP + 256;
constexpr int L_YM = L_HP + 128;
constexpr int L_RED = L_YM + 448 * 4;
constexpr int L_ML = L_RED + NSW * 2048;
constexpr int L_O = L_ML + 128;
constexpr int L_FIN = L_O + 64;
constexpr int L_XB = L_FIN + 256;
constexpr int L_FLAG = L_XB + 64;
constexpr int L_END = L_FLAG + 64;

__global__ __launch_bounds__(512, 2) void fused_decode(Params p) {
  __shared__ __attribute__((aligned(16))) char lds[L_END];
  const int tid = threadIdx.x, lane = tid & 63;
  const int w = __builtin_amdgcn_readfirstlane(tid >> 6);
  const int bid = blockIdx.x, g = bid & 7, s = bid >> 3;
  const int lg = lane >> 4, lc = lane & 15;
#define STAMP(i)
#define STAMPB(i, v)
  STAMP(0);
  const auto rW = __builtin_amdgcn_make_buffer_rsrc(p.ws, 0, WS_END, 0x00020000);
  u64 epn = 0;

  u32 tag_lo = 0, tag_hi = 0;
  u32* const cnt = (u32*)(lds + L_FLAG);
  auto lds_arrive = [&](int i) -> u32 {
    asm volatile("s_waitcnt lgkmcnt(0)" ::: "memory");
    u32 r = 0;
    if (lane == 0) r = __hip_atomic_fetch_add(cnt + i, 1u, __ATOMIC_RELAXED, __HIP_MEMORY_SCOPE_WORKGROUP);
    r = __builtin_amdgcn_readfirstlane(r);
    MEMFENCE();
    return r;
  };
  float* const xn = (float*)(lds + L_XN);
  if (w < 4 && !(VAR & 256)) {
    __builtin_amdgcn_s_setprio(HPRIO);
    const auto rX = __builtin_amdgcn_make_buffer_rsrc((void*)p.x, 0, D * 2, 0x00020000);
    const auto rWq = __builtin_amdgcn_make_buffer_rsrc((void*)p.wq, 0, (ABL & 8) ? 0 : D * HQ * DH * 2, 0x00020000);
    const u32x2 ep = __builtin_amdgcn_raw_buffer_load_b64(rW, WS_EPOCH, 0, AUX_SC1);
    const u32x4 xr = __builtin_amdgcn_raw_buffer_load_b128(rX, lane * 16, 0, 0);
    {
      const u32x4 sv = {SENT, SENT, SENT, SENT};
      const int c = (w & 1) * 16 + (lane >> 2);
      u32 off = w < 2 ? WS_OA + ((g * 32 + c) * 32 + s) * 128 + (lane & 3) * 16 : WS_YC + (c * NWG + bid) * 64 + (lane & 3) * 16;
      if (w == 3 && lane >= 48) off = OOB;
      if (!(VAR & 16)) __builtin_amdgcn_raw_buffer_store_b128(sv, rW, off, 0, AUX_SC1);
      if (!(VAR & 16)) __builtin_amdgcn_raw_buffer_store_b128(sv, rW, lane < 8 ? WS_OA + ((g * 32 + w * 8 + lane) * 32 + s) * 128 + 64 : OOB, 0, AUX_SC1);
    }
    MEMFENCE();
    u32x4 wq[4];
    const u32 wqo = (w * 32 + (lane >> 1)) * 8192 + (g * 512 + s * 16) * 2 + (lane & 1) * 16;
#pragma unroll
    for (int i = 0; i < 4; ++i) wq[i] = __builtin_amdgcn_raw_buffer_load_b128(rWq, wqo + i * 128 * 8192, 0, WQAUX);
    if (tid < 16) cnt[tid] = 0;
    bar();
    MEMFENCE(); SCHED();
    {
      float xv[8];
#pragma unroll
      for (int e = 0; e < 4; ++e) { xv[2 * e] = bf2f_lo(xr[e]); xv[2 * e + 1] = bf2f_hi(xr[e]); }
      STAMPB(1, xv[0]);
      const float mean = wave_sum(((xv[0] + xv[1]) + (xv[2] + xv[3])) + ((xv[4] + xv[5]) + (xv[6] + xv[7]))) * (1.f / D);
      float sq = 0.f;
#pragma unroll
      for (int e = 0; e < 8; ++e) { const float d = xv[e] - mean; sq = fmaf(d, d, sq); }
      sq = lane < 56 ? sq : 0.f;
      const float var = wave_sum(sq) * (1.f / D);
      const float mb = bfr(mean), den = bfr(__builtin_amdgcn_sqrtf(bfr(bfr(var) + 1e-5f)));
      const float rden = lane < 56 ? __builtin_amdgcn_rcpf(den) : 0.f;
      f32x4 o[2];
#pragma unroll
      for (int e = 0; e < 8; ++e) o[e >> 2][e & 3] = bfr(bfr(xv[e] - mb) * rden);
      *(f32x4*)(xn + lane * 8) = o[0]; *(f32x4*)(xn + lane * 8 + 4) = o[1];
    }
    float a[8];
#pragma unroll
    for (int e = 0; e < 8; ++e) a[e] = 0.f;
#pragma unroll
    for (int i = 0; i < 4; ++i) { const float xx = xn[32 * (w + 4 * i) + (lane >> 1)];
#pragma unroll
      for (int e = 0; e < 4; ++e) { a[2 * e] = fmaf(xx, bf2f_lo(wq[i][e]), a[2 * e]); a[2 * e + 1] = fmaf(xx, bf2f_hi(wq[i][e]), a[2 * e + 1]); } }
    STAMPB(2, a[7]);
#pragma unroll
    for (int e = 0; e < 4; ++e) a[e] = t32_sum(a[e], a[e + 4]);
#pragma unroll
    for (int e = 0; e < 2; ++e) a[e] = t16_sum(a[e], a[e + 2]);
    { const float t1 = a[0] + dpp<XOR8>(a[0]), t2 = a[1] + dpp<XOR8>(a[1]); float t = (lane & 8) ? t2 : t1;
      t += dpp<XOR2>(t); t += dpp<XOR7>(dpp<XOR3>(t));
      if (!(lane & 6)) ((float*)(lds + L_QP))[w * 16 + (lane & 1) * 8 + (lane >> 3)] = t; }
    const u64 ep64 = ((u64)(u32)__builtin_amdgcn_readfirstlane(ep[1]) << 32) | (u32)__builtin_amdgcn_readfirstlane(ep[0]);
    epn = ep64 * 6364136223846793005ull + 1442695040888963407ull;
    tag_lo = (u32)epn << 16; tag_hi = (u32)(epn >> 16);
    if (lds_arrive(0) == 3) {
      const float* qp = (const float*)(lds + L_QP) + lc;
      const float q = (qp[0] + qp[16]) + (qp[32] + qp[48]);
      const u32x2 gr = {tag_lo | (pk_bf16(q, 0.f) & 0xffffu), tag_hi};
      __builtin_amdgcn_raw_buffer_store_b64(gr, rW, lane < 16 ? WS_QG + (g * 512 + s * 16 + lane) * 8 : OOB, 0, AUX_SC1);
    }
    MEMFENCE();
    STAMPB(3, lane);
    if (QDELAY) __builtin_amdgcn_s_sleep(QDELAY);
  }
  if (!(VAR & 64) || w >= 4) {
    u32x4 kb[2][8], vb[2][8];
    const u32 koff = KPAT == 5 ? (4 * (lc & 3) + (lc >> 2)) * 512 + lg * 16
                   : KPAT == 8 ? (lane >> 2) * 512 + (lane & 3) * 16 : KPAT == 1 ? lg * 512 + lc * 16 : (lane >> 3) * 512 + ((lane >> 2) & 1) * 256 + (lane & 3) * 16;
    const u32 voff = lg * 512 + lc * 16;
    const u32 rot = ROT ? (((u32)bid * 0x9E3779B1u) >> 26) * 2 : 0u;
    auto toff = [&](u32 i) -> u32 { i = (i + rot) & (NTW - 1); return g * (u32)(SK * 512) + (((((i >> LOGB) * NSPLIT + s) << LOGB) + (i & ((1u << LOGB) - 1))) << 13); };
    auto ptile = [&](u32 pr, u32 d) -> u32 { return (VAR & 4) ? (pr >> 2) * 8 + d * 4 + (pr & 3) : 2 * pr + d; };
    auto loadK = [&](int d, u32 so, u32 nrec) {
      const auto rK = __builtin_amdgcn_make_buffer_rsrc((void*)p.k, 0, nrec, 0x00020000);
      u32 o = koff; asm volatile("" : "+v"(o));
#pragma unroll
      for (int j = 0; j < 8; ++j) {
        if (KPAT == 5 || KPAT == 8) kb[d][j] = __builtin_amdgcn_raw_buffer_load_b128(rK, o + j * 64, so, AUX_NT);
        else if (KPAT == 1) kb[d][j] = __builtin_amdgcn_raw_buffer_load_b128(rK, o + ((j >> 1) & 1) * 2048 + (j & 1) * 256, so + (j >> 2) * 4096, AUX_NT);
        else kb[d][j] = __builtin_amdgcn_raw_buffer_load_b128(rK, o + (j >> 1) * 64, so + (j & 1) * 4096, AUX_NT);
      }
      MEMFENCE(); SCHED();
    };
    auto loadV = [&](int d, u32 so, u32 nrec) {
      const auto rV = __builtin_amdgcn_make_buffer_rsrc((void*)p.v, 0, nrec, 0x00020000);
      u32 o = voff; asm volatile("" : "+v"(o));
#pragma unroll
      for (int j = 0; j < 8; ++j) vb[d][j] = __builtin_amdgcn_raw_buffer_load_b128(rV, o + ((j >> 1) & 1) * 2048 + (j & 1) * 256, so + (j >> 2) * 4096, AUX_NT);
      MEMFENCE(); SCHED();
    };
    const u32 pair0 = (VAR & 64) ? (w & 3) : w ^ 4;
    constexpr int NMAIN = OPT;
    const u32 npair = (VAR & 64) ? 16 : (w >= 4 && w < 4 + NMAIN) ? 1 + 56 / NMAIN : 1;
    if (w >= 4 && !(VAR & 256)) bar();
    const u32 qgoff = WS_QG + (g * 512 + (w & 3) * 128 + lane * 2) * 8;
    bool qdone = w >= 4;
    u32x4 q = {0u, 0u, 0u, 0u};
    auto poll = [&]() { if (w < 4) asm volatile("global_load_dwordx4 %0, %1, %2 sc1" : "=v"(q) : "v"(qgoff), "s"(p.ws) : "memory"); SCHED(); };
    auto accept = [&]() {
      if (!qdone && __all((q[0] & 0xffff0000u) == tag_lo && q[1] == tag_hi && (q[2] & 0xffff0000u) == tag_lo && q[3] == tag_hi)) {
        *(u32*)(lds + L_Q + (w >> 1) * QSTR + (w & 1) * 256 + lane * 4) = (q[0] & 0xffffu) | (q[2] << 16);
        lds_arrive(1);
        qdone = true;
        STAMPB(4, lane);
      }
    };
    auto check = [&]() { if (w < 4) { asm volatile("s_waitcnt vmcnt(8)" : "+v"(q) :: "memory"); accept(); } MEMFENCE(); SCHED(); };
    {
      const u32 so0 = toff(ptile(pair0, 0)), so1 = toff(ptile(pair0, 1));
      if ((VAR & 8) && !qdone) { lds_arrive(1); qdone = true; }
      if (NPOLL & 1) poll();
      if (w >= 4) STAMPB(2, lane);
      loadK(0, so0, 0x10000000u); if (NPOLL & 1) check();
      if (w >= 4) STAMPB(3, lane);
      if (NPOLL & 2) poll();
      loadV(0, so0, 0x10000000u); if (NPOLL & 2) check();
      if (w >= 4) STAMPB(4, lane);
      if (NPOLL & 4) poll();
      loadK(1, so1, 0x10000000u); if (NPOLL & 4) check();
      if (NPOLL & 8) poll();
      loadV(1, so1, 0x10000000u); if (NPOLL & 8) check();
      for (u32 spins = 0; !qdone; ++spins) {
        asm volatile("global_load_dwordx4 %0, %1, %2 sc1\n\ts_waitcnt vmcnt(0)" : "=&v"(q) : "v"(qgoff), "s"(p.ws) : "memory");
        accept();
        if (spins >= SPIN_CAP && !qdone) { lds_arrive(1); qdone = true; }
      }
    }
    __builtin_amdgcn_s_setprio(0);
    if (w >= 4) STAMPB(1, lane);
    if (!(VAR & 64)) while (__hip_atomic_load(cnt + 1, __ATOMIC_RELAXED, __HIP_MEMORY_SCOPE_WORKGROUP) != 4) __builtin_amdgcn_s_sleep(2);
    MEMFENCE();
    STAMP(1); STAMPB(5, lane);
    const char* qfp = lds + L_Q + (lc & 1) * QSTR + lg * 16;
    constexpr float C2 = 0.0625f * 1.4426950408889634f;
    float m = -INFINITY, l = 0.f;
    f32x4 acc[16];
#pragma unroll
    for (int i = 0; i < 16; ++i) acc[i] = f32x4{0.f, 0.f, 0.f, 0.f};
    auto qk = [&](int d) -> f32x4 {
      f32x4 sc = {0.f, 0.f, 0.f, 0.f};
#pragma unroll
      for (int j = 0; j < 8; ++j) if (VAR & 1) sc = __builtin_bit_cast(f32x4, __builtin_bit_cast(u32x4, sc) ^ kb[d][j]); else sc = __builtin_amdgcn_mfma_f32_16x16x32_bf16(__builtin_bit_cast(bf16x8, kb[d][j]), *(const bf16x8*)(qfp + j * 64), sc, 0, 0, 0);
      asm volatile("" : "+v"(sc) :: "memory"); SCHED();
      return sc;
    };
    auto pv = [&](int d, f32x4 sc) {
      if (VAR & 2) { u32x4 t = __builtin_bit_cast(u32x4, sc) ^ __builtin_bit_cast(u32x4, acc[0]);
#pragma unroll
        for (int j = 0; j < 8; ++j) t ^= vb[d][j];
        acc[0] = __builtin_bit_cast(f32x4, t); asm volatile("" : "+v"(acc[0]) :: "memory"); SCHED(); return; }
      float t[4];
      { const u32 a = pk_bf16(sc[0], sc[1]), b = pk_bf16(sc[2], sc[3]);
        t[0] = bf2f_lo(a) * C2; t[1] = bf2f_hi(a) * C2; t[2] = bf2f_lo(b) * C2; t[3] = bf2f_hi(b) * C2; }
      const float tm = x32_max(x16_max(fmaxf(fmaxf(t[0], t[1]), fmaxf(t[2], t[3]))));
      if (__any(tm > m)) {
        const float mn = fmaxf(m, tm), al = ex2(m - mn);
        m = mn; l *= al;
        const float a0 = dpp<0xA0>(al), a1 = dpp<0xF5>(al);
#pragma unroll
        for (int i = 0; i < 16; ++i) { acc[i][0] *= a0; acc[i][1] *= a1; }
      }
      const float p0 = ex2(t[0] - m), p1 = ex2(t[1] - m), p2 = ex2(t[2] - m), p3 = ex2(t[3] - m);
      l += (p0 + p1) + (p2 + p3);
      const u32x2 pp = {pk_bf16(p0, p1), pk_bf16(p2, p3)};
      const s16x4 pf = __builtin_bit_cast(s16x4, pp);
#pragma unroll
      for (int hf = 0; hf < 2; ++hf)
#pragma unroll
        for (int e = 0; e < 8; ++e) {
          const u32 sel = (e & 1) ? 0x07060302u : 0x05040100u;
          const u32x2 bb = {__builtin_amdgcn_perm(vb[d][2 + hf][e >> 1], vb[d][hf][e >> 1], sel), __builtin_amdgcn_perm(vb[d][6 + hf][e >> 1], vb[d][4 + hf][e >> 1], sel)};
          acc[hf * 8 + e] = __builtin_amdgcn_mfma_f32_16x16x16bf16_1k(pf, __builtin_bit_cast(s16x4, bb), acc[hf * 8 + e], 0, 0, 0);
        }
      MEMFENCE(); SCHED();
    };
    for (u32 r = 1; r < npair; ++r) {
      const u32 tp = (VAR & 64) ? (w & 3) + r * 4 : w + 4 + (r - 1) * NMAIN;
#pragma unroll
      for (int d = 0; d < 2; ++d) {
        const u32 so = toff(ptile(tp, d));
        const f32x4 sc = qk(d);
        loadK(d, so, 0x10000000u); pv(d, sc);
        loadV(d, so, 0x10000000u);
      }
    }
#pragma unroll
    for (int d = 0; d < 2; ++d) { const f32x4 sc = qk(d); pv(d, sc);
    }
    STAMP(2);
    l = x32_sum(x16_sum(l));
    float* red = (float*)(lds + L_RED) + w * 512 + (lg & 1) * 256 + (lg >> 1) * 128 + lc * 8;
    float o[8];
#pragma unroll
    for (int e = 0; e < 8; ++e) { const float lo = (lg & 1) ? acc[e][1] : acc[e][0], hi = (lg & 1) ? acc[8 + e][1] : acc[8 + e][0]; o[e] = (lg >> 1) ? hi : lo; }
    *(f32x4*)red = f32x4{o[0], o[1], o[2], o[3]}; *(f32x4*)(red + 4) = f32x4{o[4], o[5], o[6], o[7]};
    if (lane < 2) *(float2*)((float*)(lds + L_ML) + w * 4 + lane * 2) = make_float2(m, l);
  }
  if (w < 4 && !(VAR & 128)) {
    const auto rWo = __builtin_amdgcn_make_buffer_rsrc((void*)p.wo, 0, (ABL & 4) ? 0 : D * HQ * DH * 2, 0x00020000);
    const auto rW1 = __builtin_amdgcn_make_buffer_rsrc((void*)p.w1, 0, (ABL & 1) ? 0 : D * FF * 2, 0x00020000);
    const auto rW2 = __builtin_amdgcn_make_buffer_rsrc((void*)p.w2, 0, (ABL & 2) ? 0 : D * FF * 2, 0x00020000);
    const auto rB1 = __builtin_amdgcn_make_buffer_rsrc((void*)p.b1, 0, FF * 2, 0x00020000);
    const int lcol = g + 8 * (s >> 3);
    const bool has_mlp = lcol < 28;
    const int u = (has_mlp ? lcol : 0) * 8 + (s & 7);
    const u32 rowoff = lane < 56 ? lane * 16 : OOB;
    u32x4 w1r[2], w2r[2], wor[4];
#pragma unroll
    for (int i = 0; i < 2; ++i) w1r[i] = __builtin_amdgcn_raw_buffer_load_b128(rW1, has_mlp ? (64 * (w + 4 * i) + lane) * (FF * 2) + u * 16 : OOB, 0, W1AUX);
#pragma unroll
    for (int i = 0; i < 2; ++i) w2r[i] = __builtin_amdgcn_raw_buffer_load_b128(rW2, has_mlp ? rowoff : OOB, (u * 8 + w * 2 + i) * 896, AUX_NT);
#pragma unroll
    for (int i = 0; i < 4; ++i) wor[i] = __builtin_amdgcn_raw_buffer_load_b128(rWo, rowoff, (g * 512 + s * 16 + w * 4 + i) * 896, AUX_NT);
    const u32 b1r = __builtin_amdgcn_raw_buffer_load_b16(rB1, (u * 8 + (lane & 7)) * 2, 0, 0);
    float h[8];
#pragma unroll
    for (int e = 0; e < 8; ++e) h[e] = 0.f;
#pragma unroll
    for (int i = 0; i < 2; ++i) { const float xx = xn[64 * (w + 4 * i) + lane];
#pragma unroll
      for (int e = 0; e < 4; ++e) { h[2 * e] = fmaf(xx, bf2f_lo(w1r[i][e]), h[2 * e]); h[2 * e + 1] = fmaf(xx, bf2f_hi(w1r[i][e]), h[2 * e + 1]); } }
#pragma unroll
    for (int e = 0; e < 8; ++e) h[e] = wave_sum(h[e]);
    if (lane == 0) { float* hp = (float*)(lds + L_HP) + w * 8;
      *(f32x4*)hp = f32x4{h[0], h[1], h[2], h[3]}; *(f32x4*)(hp + 4) = f32x4{h[4], h[5], h[6], h[7]}; }
    if (lane < 56) {
#pragma unroll
      for (int i = 0; i < 2; ++i) *(u32x4*)(lds + L_W2 + (w * 2 + i) * 896 + lane * 16) = w2r[i];
#pragma unroll
      for (int i = 0; i < 4; ++i) *(u32x4*)(lds + L_WO + (w * 4 + i) * 896 + lane * 16) = wor[i];
    }
    if (lds_arrive(2) == 3) {
      const float* hp = (const float*)(lds + L_HP) + (lane & 7);
      float hv = bfr(bfr((hp[0] + hp[8]) + (hp[16] + hp[24])) + bf2f_lo(b1r));
      hv = bfr(0.5f * hv * (1.f + tanhf(0.7978845608028654f * (hv + 0.044715f * hv * hv * hv))));
      hv = has_mlp ? hv : 0.f;
      float ym[8];
#pragma unroll
      for (int e = 0; e < 8; ++e) ym[e] = 0.f;
#pragma unroll
      for (int r = 0; r < 8; ++r) {
        const float hr = __builtin_bit_cast(float, __builtin_amdgcn_readlane(__builtin_bit_cast(int, hv), r));
        const u32x4 wv = *(const u32x4*)(lds + L_W2 + r * 896 + (lane < 56 ? lane : 0) * 16);
#pragma unroll
        for (int e = 0; e < 4; ++e) { ym[2 * e] = fmaf(hr, bf2f_lo(wv[e]), ym[2 * e]); ym[2 * e + 1] = fmaf(hr, bf2f_hi(wv[e]), ym[2 * e + 1]); }
      }
      if (lane < 56) { float* y = (float*)(lds + L_YM) + lane * 8;
        *(f32x4*)y = f32x4{ym[0], ym[1], ym[2], ym[3]}; *(f32x4*)(y + 4) = f32x4{ym[4], ym[5], ym[6], ym[7]}; }
    }
    if (bid < NFIN) {
      for (u32 spins = 0; spins < SPIN_CAP; ++spins) {
        MEMFENCE();
        const u32x2 t = __builtin_amdgcn_raw_buffer_load_b64(rW, WS_QG + (w * 64 + lane) * 128, 0, AUX_SC1);
        if (__all((t[0] & 0xffff0000u) == tag_lo && t[1] == tag_hi)) break;
      }
      if (tid < 16) { const int col = bid * 16 + tid;
        ((float*)(lds + L_XB))[tid] = __bfloat162float(((const __hip_bfloat16*)p.x)[col]) + __bfloat162float(((const __hip_bfloat16*)p.b2)[col]); }
    }
    STAMPB(6, lane);
  }
  bar();
  STAMP(3);
  if (VAR & 32) {
    return; }
  if (w < 3) {
    const float* ml = (const float*)(lds + L_ML);
    if (w < 2) {
      float mw[NSW], M = -INFINITY;
#pragma unroll
      for (int i = 0; i < NSW; ++i) { mw[i] = ml[i * 4 + w * 2]; M = fmaxf(M, mw[i]); }
      const float* red = (const float*)(lds + L_RED) + w * 256 + lane * 4;
      f32x4 o = {0.f, 0.f, 0.f, 0.f};
#pragma unroll
      for (int i = 0; i < NSW; ++i) o += *(const f32x4*)(red + i * 512) * ex2(mw[i] - M);
      const u32x4 ov = {san(o[0]), san(o[1]), san(o[2]), san(o[3])};
      __builtin_amdgcn_raw_buffer_store_b128(ov, rW, WS_OA + ((g * 32 + w * 16 + (lane >> 2)) * 32 + s) * 128 + (lane & 3) * 16, 0, AUX_SC1);
    } else {
      const int h2 = lg & 1;
      float M = -INFINITY, L = 0.f;
#pragma unroll
      for (int i = 0; i < NSW; ++i) M = fmaxf(M, ml[i * 4 + h2 * 2]);
#pragma unroll
      for (int i = 0; i < NSW; ++i) L = fmaf(ml[i * 4 + h2 * 2 + 1], ex2(ml[i * 4 + h2 * 2] - M), L);
      const u32x4 ov = {san(M), san(L), san(M), san(L)};
      __builtin_amdgcn_raw_buffer_store_b128(ov, rW, lane < 32 ? WS_OA + ((g * 32 + lane) * 32 + s) * 128 + 64 : OOB, 0, AUX_SC1);
    }
  }
  MEMFENCE();
  STAMP(4);
  if (w == 4) {
    const u32 roff = WS_OA + ((g * 32 + s) * 32 + (lane >> 2)) * 128;
    u32x4 ov[2]; u32x2 mv[2];
    bool ok0 = false, ok1 = false;
    for (u32 spins = 0; spins < SPIN_CAP; ++spins) {
      MEMFENCE();
      if (!ok0) { ov[0] = __builtin_amdgcn_raw_buffer_load_b128(rW, roff + (lane & 3) * 16, 0, AUX_SC1); mv[0] = __builtin_amdgcn_raw_buffer_load_b64(rW, roff + 64, 0, AUX_SC1); }
      if (!ok1) { ov[1] = __builtin_amdgcn_raw_buffer_load_b128(rW, roff + 2048 + (lane & 3) * 16, 0, AUX_SC1); mv[1] = __builtin_amdgcn_raw_buffer_load_b64(rW, roff + 2048 + 64, 0, AUX_SC1); }
      ok0 = valid4(ov[0]) & (mv[0][0] != SENT) & (mv[0][1] != SENT);
      ok1 = valid4(ov[1]) & (mv[1][0] != SENT) & (mv[1][1] != SENT);
      const int miss = __popcll(__ballot(!ok0)) + __popcll(__ballot(!ok1));
      if (miss == 0) break;
      for (int i = 8; i < miss; i += 4) __builtin_amdgcn_s_sleep(PSLEEP);
    }
    const float m0 = __uint_as_float(mv[0][0]), m1 = __uint_as_float(mv[1][0]);
    float M = fmaxf(m0, m1);
    M = fmaxf(M, dpp<ROR4>(M)); M = fmaxf(M, dpp<XOR8>(M)); M = x32_max(x16_max(M));
    const float w0 = ex2(m0 - M), w1 = ex2(m1 - M);
    float L = fmaf(w0, __uint_as_float(mv[0][1]), w1 * __uint_as_float(mv[1][1]));
    L += dpp<ROR4>(L); L += dpp<XOR8>(L); L = x32_sum(x16_sum(L));
    float o[4];
#pragma unroll
    for (int e = 0; e < 4; ++e) o[e] = fmaf(w0, __uint_as_float(ov[0][e]), w1 * __uint_as_float(ov[1][e]));
    o[0] = t32_sum(o[0], o[2]); o[1] = t32_sum(o[1], o[3]);
    float t = t16_sum(o[0], o[1]);
    t += dpp<ROR4>(t); t += dpp<XOR8>(t);
    if (!(lane & 12)) ((float*)(lds + L_O))[(lane & 3) * 4 + lg] = bfr(t * __builtin_amdgcn_rcpf(L));
  }
  bar();
  STAMP(5);
  if (w < 4) {
    const int l2 = lane < 56 ? lane : 0;
    const u32* wol = (const u32*)(lds + L_WO) + w * 56 + l2;
    const float* ol = (const float*)(lds + L_O);
    const float2 ym = *(const float2*)((const float*)(lds + L_YM) + w * 112 + l2 * 2);
    float y0 = ym.x, y1 = ym.y;
#pragma unroll
    for (int r = 0; r < 16; ++r) { const float orr = ol[r]; const u32 ww = wol[r * 224];
      y0 = fmaf(orr, bf2f_lo(ww), y0); y1 = fmaf(orr, bf2f_hi(ww), y1); }
    const float z0 = dpp<XOR1>(y0), z1 = dpp<XOR1>(y1);
    const int col = w * 112 + lane * 2;
    const u32x4 ov = {san(y0), san(y1), san(z0), san(z1)};
    __builtin_amdgcn_raw_buffer_store_b128(ov, rW, (lane < 56 && !(lane & 1)) ? WS_YC + ((col >> 4) * NWG + bid) * 64 + (col & 15) * 4 : OOB, 0, AUX_SC1);
  }
  MEMFENCE();
  STAMP(6);
  if (bid < NFIN) {
    if (w >= 4) {
      const u32 off = WS_YC + (bid * NWG + (w - 4) * 64) * 64 + lane * 16;
      u32x4 yv[4];
      bool ok[4] = {false, false, false, false};
      for (u32 spins = 0; spins < SPIN_CAP; ++spins) {
        MEMFENCE();
#pragma unroll
        for (int i = 0; i < 4; ++i) if (!ok[i]) yv[i] = __builtin_amdgcn_raw_buffer_load_b128(rW, off + i * 1024, 0, AUX_SC1);
        int miss = 0;
#pragma unroll
        for (int i = 0; i < 4; ++i) { ok[i] = valid4(yv[i]); miss += __popcll(__ballot(!ok[i])); }
        if (miss == 0) break;
        for (int i = 32; i < miss; i += 16) __builtin_amdgcn_s_sleep(PSLEEP);
      }
      float v[4];
#pragma unroll
      for (int e = 0; e < 4; ++e) v[e] = (__uint_as_float(yv[0][e]) + __uint_as_float(yv[1][e])) + (__uint_as_float(yv[2][e]) + __uint_as_float(yv[3][e]));
      v[0] = t32_sum(v[0], v[2]); v[1] = t32_sum(v[1], v[3]);
      float t = t16_sum(v[0], v[1]);
      t += dpp<ROR4>(t); t += dpp<XOR8>(t);
      if (!(lane & 12)) ((float*)(lds + L_FIN))[(w - 4) * 16 + (lane & 3) * 4 + lg] = t;
    }
    bar();
    if (tid < 16) {
      const float* fin = (const float*)(lds + L_FIN) + tid;
      const float r = ((const float*)(lds + L_XB))[tid] + ((fin[0] + fin[16]) + (fin[32] + fin[48]));
      ((unsigned short*)p.out)[bid * 16 + tid] = (unsigned short)(pk_bf16(r, 0.f) & 0xffffu);
      if (bid == 0 && tid == 0) { const u32x2 e2 = {(u32)epn, (u32)(epn >> 32)}; __builtin_amdgcn_raw_buffer_store_b64(e2, rW, WS_EPOCH, 0, AUX_SC1); }
    }
  }
}

extern "C" void kernel_launch(void* const* d_in, const int* in_sizes, int n_in, void* d_out, int out_size, void* d_ws, size_t ws_size, hipStream_t stream) {
  Params p;
  p.x = d_in[0]; p.k = d_in[1]; p.v = d_in[2]; p.wq = d_in[3]; p.wo = d_in[4];
  p.w1 = d_in[6]; p.b1 = d_in[7]; p.w2 = d_in[8]; p.b2 = d_in[9];
  p.out = d_out; p.ws = d_ws;
  hipLaunchKernelGGL(fused_decode, dim3(NWG), dim3(512), 0, stream, p);
}
```

```cpp
#include <hip/hip_runtime.h>
#include <hip/hip_bf16.h>
#include <stdint.h>

typedef unsigned u32;
typedef unsigned long long u64;
typedef u32 u32x4 __attribute__((ext_vector_type(4)));
typedef u32 u32x2 __attribute__((ext_vector_type(2)));
typedef float f32x4 __attribute__((ext_vector_type(4)));
typedef __bf16 bf16x8 __attribute__((ext_vector_type(8)));
typedef __bf16 bf16x2 __attribute__((ext_vector_type(2)));
typedef short s16x4 __attribute__((ext_vector_type(4)));
#define DI __device__ __forceinline__

#define STAMPS 0
#define TMASK 0x13a
#define QT 400
#define KVT 0
#define CHT 20
#define PSLEEP 2
#define ABL 0
#define VAR 0
#define PRET 2
#define DLY 7900
#define CSLEEP 6
#define PTHR 8
#define PACE 1
#define KA 26
#define KB 42
#define KC 44
#define FCD 0
#define HEADMAP 1
#define FCG 400
#define HPRIO 3

constexpr int D = 448, HQ = 16, SK = 65536, DH = 256, FF = 1792;
constexpr int NWG = 256, NWAVE_TOTAL = 8;
constexpr int NPAIR = 64;
constexpr int NFIN = 28;
constexpr u32 WS_EPOCH = 0;
constexpr u32 WS_PR = 2048;
constexpr u32 WS_QG = 4096;
constexpr u32 WS_RA = WS_QG + 8 * 512 * 8;
constexpr u32 RA_BLK = 32 * 80;
constexpr u32 WS_RC = WS_RA + NWG * RA_BLK;
constexpr u32 RC_BLK = 128 * 128;
constexpr u32 WS_END = WS_RC + NFIN * RC_BLK;
constexpr u32 WS_STAMP = 2u << 20;
static_assert(WS_END <= WS_STAMP, "");
constexpr int AUX_NT = 2, AUX_SC1 = 16;
constexpr u32 SPIN_CAP = 1u << 15;
constexpr u32 OOB = 0x7fffff00u;

#define MEMFENCE() asm volatile("" ::: "memory")
#define SCHED() __builtin_amdgcn_sched_barrier(0)

DI float bf2f_lo(u32 x) { return __builtin_bit_cast(float, x << 16); }
DI float bf2f_hi(u32 x) { return __builtin_bit_cast(float, x & 0xffff0000u); }
typedef float f32x2 __attribute__((ext_vector_type(2)));
DI u32 pk_bf16(float a, float b) { const f32x2 f = {a, b}; return __builtin_bit_cast(u32, __builtin_convertvector(f, bf16x2)); }
DI float bfr(float a) { return bf2f_lo(pk_bf16(a, 0.f)); }
template <int CTRL> DI float dpp(float x) {
  return __builtin_bit_cast(float, __builtin_amdgcn_mov_dpp(__builtin_bit_cast(int, x), CTRL, 0xf, 0xf, true));
}
constexpr int XOR1 = 0xB1, XOR2 = 0x4E, XOR3 = 0x1B, XOR7 = 0x141, XOR8 = 0x128, ROR4 = 0x124;
DI float t32_sum(float u, float v) { auto s = __builtin_amdgcn_permlane32_swap(__float_as_uint(u), __float_as_uint(v), false, false); return __uint_as_float(s[0]) + __uint_as_float(s[1]); }
DI float t16_sum(float u, float v) { auto s = __builtin_amdgcn_permlane16_swap(__float_as_uint(u), __float_as_uint(v), false, false); return __uint_as_float(s[0]) + __uint_as_float(s[1]); }
DI float x16_sum(float x) { return t16_sum(x, x); }
DI float x32_sum(float x) { return t32_sum(x, x); }
DI float x16_max(float x) { auto s = __builtin_amdgcn_permlane16_swap(__float_as_uint(x), __float_as_uint(x), false, false); return fmaxf(__uint_as_float(s[0]), __uint_as_float(s[1])); }
DI float x32_max(float x) { auto s = __builtin_amdgcn_permlane32_swap(__float_as_uint(x), __float_as_uint(x), false, false); return fmaxf(__uint_as_float(s[0]), __uint_as_float(s[1])); }
DI float row_sum(float x) { x += dpp<XOR1>(x); x += dpp<XOR2>(x); x += dpp<XOR7>(x); x += dpp<XOR8>(x); return x; }
DI float wave_sum(float x) { return x32_sum(x16_sum(row_sum(x))); }
template <int CTRL> DI u32 dppu(u32 x) { return (u32)__builtin_amdgcn_mov_dpp((int)x, CTRL, 0xf, 0xf, true); }
DI u32 rotl(u32 x, u32 r) { return __builtin_amdgcn_alignbit(x, x, 32u - r); }
DI void chk(u32& ca, u32& cb, u32 v, u32 i) { ca += v; cb ^= rotl(v, i + 1); }
DI void bar() { asm volatile("s_waitcnt lgkmcnt(0)\n\ts_barrier" ::: "memory"); }
DI float dot2(u32 a, u32 b, float c) { return __builtin_amdgcn_fdot2_f32_bf16(__builtin_bit_cast(bf16x2, a), __builtin_bit_cast(bf16x2, b), c, false); }
DI float ex2(float x) { return __builtin_amdgcn_exp2f(x); }
template <u32 M> DI u32 pdep(u32 x) {
  u32 r = 0, k = 0;
#pragma unroll
  for (int b = 0; b < 12; ++b) if ((M >> b) & 1) { r |= ((x >> k) & 1u) << b; ++k; }
  return r;
}

struct Params {
  const void* x; const void* k; const void* v; const void* wq; const void* wo;
  const void* w1; const void* b1; const void* w2; const void* b2;
  void* out; void* ws;
};

constexpr int L_XN = 0;
constexpr int QSTR = 576;
constexpr int L_Q = L_XN + 2048;
constexpr int L_WO = L_Q + 2 * QSTR;
constexpr int L_QP = L_WO + 64 * 896;
constexpr int L_YM = L_QP + 512;
constexpr int L_RED = L_YM + 4 * 448 * 4;
constexpr int L_ML = L_RED + 8 * 2048;
constexpr int L_O = L_ML + 128;
constexpr int L_FIN = L_O + 128;
constexpr int L_XB = L_FIN + 512;
constexpr int L_EP = L_XB + 64;
constexpr int L_CNT = L_EP + 16;
constexpr int L_END = L_CNT + 64;
enum { C_QPART, C_QRDY, C_HP, C_CHORE, C_TURN, C_PART, C_MERGED, C_MARK, C_TEND };

__global__ __launch_bounds__(512, 2) void fused_decode(Params p) {
  __shared__ __attribute__((aligned(16))) char lds[L_END];
  const int tid = threadIdx.x, lane = tid & 63;
  const int w = __builtin_amdgcn_readfirstlane(tid >> 6);
  const int bid = blockIdx.x;
  const int g = HEADMAP == 0 ? bid & 7 : ((bid >> 6) << 1) | ((bid >> 2) & 1);
  const int s = HEADMAP == 0 ? bid >> 3 : (((bid >> 4) & 3) << 3) | ((bid & 3) << 1) | ((bid >> 3) & 1);
  const int lg = lane >> 4, lc = lane & 15;
#define STAMPC(i)
#define STAMP(i)
#define STAMPB(i, v)
  STAMP(0);
  const auto rW = __builtin_amdgcn_make_buffer_rsrc(p.ws, 0, WS_END, 0x00020000);
  u64 epn = 0;
  u32* const cnt = (u32*)(lds + L_CNT);
  float* const xn = (float*)(lds + L_XN);
  auto lds_arrive = [&](int i) -> u32 {
    asm volatile("s_waitcnt lgkmcnt(0)" ::: "memory");
    u32 r = 0;
    if (lane == 0) r = __hip_atomic_fetch_add(cnt + i, 1u, __ATOMIC_RELAXED, __HIP_MEMORY_SCOPE_WORKGROUP);
    r = __builtin_amdgcn_readfirstlane(r);
    MEMFENCE();
    return r;
  };
  auto lds_wait = [&](int i, u32 v) { while (__hip_atomic_load(cnt + i, __ATOMIC_RELAXED, __HIP_MEMORY_SCOPE_WORKGROUP) != v) __builtin_amdgcn_s_sleep(1); MEMFENCE(); };
  const u64 tstart = __builtin_amdgcn_s_memrealtime();
  auto wait_until = [&](u32 ticks) { while ((u32)(__builtin_amdgcn_s_memrealtime() - tstart) < ticks) __builtin_amdgcn_s_sleep(1); };
  const bool q_prod = s < 16, has_mlp = s >= 16 && s < (g < 4 ? 24 : 22), cons = s >= 24;
  const int mi = (g < 4 ? g * 8 : 8 + g * 6) + s - 16;
  const int cslot = cons ? (s - 24) * 8 + g : 64 + mi;
  const int fid = (s - 24) * 8 + g;
  const bool fin = cons && fid < NFIN;
  auto gemv32 = [&](const u32x4 (&wr)[7]) {
    float a[8];
#pragma unroll
    for (int e = 0; e < 8; ++e) a[e] = 0.f;
#pragma unroll
    for (int i = 0; i < 7; ++i) { const float xx = xn[16 * (w + 4 * i) + (lane >> 2)];
#pragma unroll
      for (int e = 0; e < 4; ++e) { a[2 * e] = fmaf(xx, bf2f_lo(wr[i][e]), a[2 * e]); a[2 * e + 1] = fmaf(xx, bf2f_hi(wr[i][e]), a[2 * e + 1]); } }
#pragma unroll
    for (int e = 0; e < 4; ++e) a[e] = t32_sum(a[e], a[e + 4]);
#pragma unroll
    for (int e = 0; e < 2; ++e) a[e] = t16_sum(a[e], a[e + 2]);
    const float t1 = a[0] + dpp<XOR8>(a[0]), t2 = a[1] + dpp<XOR8>(a[1]);
    float t = (lane & 8) ? t2 : t1;
    t += dpp<XOR7>(dpp<XOR3>(t));
    if (!(lane & 4)) ((float*)(lds + L_QP))[w * 32 + (lane & 3) * 8 + (lane >> 3)] = t;
  };
  if (PRET && w < 4) {
    const u32x4 z = {0u, 0u, 0u, 0u};
    __builtin_amdgcn_raw_buffer_store_b128(z, rW, tid < 160 ? WS_RA + (g * 32 + s) * RA_BLK + tid * 16 : OOB, 0, AUX_SC1);
    if (PRET > 1 && (cons || has_mlp)) __builtin_amdgcn_raw_buffer_store_b128(z, rW, lane < 56 ? WS_RC + (w * 7 + (lane >> 3)) * RC_BLK + cslot * 128 + (lane & 7) * 16 : OOB, 0, AUX_SC1);
    MEMFENCE();
  }
  u32x4 xr, wq[7];
  u32x2 ep = {0u, 0u};
  if (w < 4 || w == 5) ep = __builtin_amdgcn_raw_buffer_load_b64(rW, WS_EPOCH, 0, AUX_SC1);
  u32 tag_lo = 0, tag_hi = 0;
  auto take_epoch = [&]() {
    asm volatile("" : "+v"(ep));
    const u64 ep64 = ((u64)(u32)__builtin_amdgcn_readfirstlane(ep[1]) << 32) | (u32)__builtin_amdgcn_readfirstlane(ep[0]);
    epn = ep64 * 6364136223846793005ull + 1442695040888963407ull;
    tag_lo = (u32)epn << 16; tag_hi = (u32)(epn >> 16);
  };
  if (w < 4) {
    __builtin_amdgcn_s_setprio(HPRIO);
    const auto rX = __builtin_amdgcn_make_buffer_rsrc((void*)p.x, 0, D * 2, 0x00020000);
    const auto rWq = __builtin_amdgcn_make_buffer_rsrc((void*)p.wq, 0, ((ABL & 8) || !q_prod) ? 0 : D * HQ * DH * 2, 0x00020000);
    xr = __builtin_amdgcn_raw_buffer_load_b128(rX, lane * 16, 0, 0);
    const u32 wqo = (w * 16 + (lane >> 2)) * 8192 + (g * 512 + s * 32) * 2 + (lane & 3) * 16;
#pragma unroll
    for (int i = 0; i < 7; ++i) wq[i] = __builtin_amdgcn_raw_buffer_load_b128(rWq, wqo, i * 64 * 8192, AUX_NT);
  }
  if (w != 4) {
    if (tid < 16) cnt[tid] = 0;
    bar();
  }
  if (__builtin_expect(w < 4, 0)) {
    {
      float xv[8];
#pragma unroll
      for (int e = 0; e < 4; ++e) { xv[2 * e] = bf2f_lo(xr[e]); xv[2 * e + 1] = bf2f_hi(xr[e]); }
      STAMPB(1, xv[0]);
      const float mean = wave_sum(((xv[0] + xv[1]) + (xv[2] + xv[3])) + ((xv[4] + xv[5]) + (xv[6] + xv[7]))) * (1.f / D);
      float sq = 0.f;
#pragma unroll
      for (int e = 0; e < 8; ++e) { const float d = xv[e] - mean; sq = fmaf(d, d, sq); }
      sq = lane < 56 ? sq : 0.f;
      const float var = wave_sum(sq) * (1.f / D);
      const float mb = bfr(mean), den = bfr(__builtin_amdgcn_sqrtf(bfr(bfr(var) + 1e-5f)));
      const float rden = lane < 56 ? __builtin_amdgcn_rcpf(den) : 0.f;
      f32x4 o[2];
#pragma unroll
      for (int e = 0; e < 8; ++e) o[e >> 2][e & 3] = bfr(bfr(xv[e] - mb) * rden);
      *(f32x4*)(xn + lane * 8) = o[0]; *(f32x4*)(xn + lane * 8 + 4) = o[1];
    }
    take_epoch();
    if (q_prod) {
      gemv32(wq);
      STAMPB(2, lane);
      if (lds_arrive(C_QPART) == 3) {
        const float* qp = (const float*)(lds + L_QP) + (lane & 31);
        const float q = (qp[0] + qp[32]) + (qp[64] + qp[96]);
        const u32x2 gr = {tag_lo | (pk_bf16(q, 0.f) & 0xffffu), tag_hi};
        __builtin_amdgcn_raw_buffer_store_b64(gr, rW, lane < 32 ? WS_QG + (g * 512 + s * 32 + lane) * 8 : OOB, 0, AUX_SC1);
      }
    }
    MEMFENCE();
    STAMPB(3, lane);
    __builtin_amdgcn_s_setprio(0);
  }

  float m = -INFINITY, l = 0.f, o[8];
  {
    const auto rK = __builtin_amdgcn_make_buffer_rsrc((void*)p.k, 0, 0x10000000, 0x00020000);
    const auto rV = __builtin_amdgcn_make_buffer_rsrc((void*)p.v, 0, 0x10000000, 0x00020000);
    u32x4 kb[2][8], vb[2][8];
    const u32 koff = (4 * (lc & 3) + (lc >> 2)) * 512 + lg * 16;
    const u32 voff = lg * 512 + lc * 16;
    const u32 tbase = g * (u32)(SK * 512) + (pdep<TMASK>(s) << 13);
    auto toff = [&](u32 i) -> u32 { return tbase + (pdep<~TMASK & 0xfff>(i) << 13); };
    auto loadK = [&](int d, u32 so) {
      u32 oo = koff; asm volatile("" : "+v"(oo));
#pragma unroll
      for (int j = 0; j < 8; ++j) kb[d][j] = __builtin_amdgcn_raw_buffer_load_b128(rK, oo + j * 64, so, AUX_NT);
      MEMFENCE(); SCHED();
    };
    auto loadV = [&](int d, u32 so) {
      u32 oo = voff; asm volatile("" : "+v"(oo));
#pragma unroll
      for (int j = 0; j < 8; ++j) vb[d][j] = __builtin_amdgcn_raw_buffer_load_b128(rV, oo + ((j >> 1) & 1) * 2048 + (j & 1) * 256, so + (j >> 2) * 4096, AUX_NT);
      MEMFENCE(); SCHED();
    };
    const u32 pair0 = w ^ 4;
    {
      const u32 so0 = toff(2 * pair0), so1 = toff(2 * pair0 + 1);
      asm volatile("" :: "s"(so0), "s"(so1), "v"(koff), "v"(voff));
      if (pair0) lds_wait(C_TURN, pair0); else if (KVT && q_prod) wait_until(KVT);
      STAMPC(1);
      if (w >= 4) STAMPB(2, lane);
      loadK(0, so0);
      if (w == 4) bar();
      loadV(0, so0); loadK(1, so1); loadV(1, so1);
      if (lane == 0) __hip_atomic_store(cnt + C_TURN, pair0 + 1, __ATOMIC_RELAXED, __HIP_MEMORY_SCOPE_WORKGROUP);
      MEMFENCE();
    }
    STAMPC(2);
    if (w >= 4) STAMPB(1, lane);
    if (w == 5) {
      take_epoch();
      if (!(VAR & 8)) {
        const u32 qgoff = WS_QG + (g * 512 + lane * 2) * 8;
        u32x4 q[4];
        bool ok[4] = {false, false, false, false};
        wait_until(QT);
        for (u32 spins = 0; spins < SPIN_CAP; ++spins) {
          MEMFENCE();
#pragma unroll
          for (int i = 0; i < 4; ++i) if (!ok[i]) q[i] = __builtin_amdgcn_raw_buffer_load_b128(rW, qgoff + i * 1024, 0, AUX_SC1);
#pragma unroll
          for (int i = 0; i < 4; ++i) ok[i] = ((q[i][0] & 0xffff0000u) == tag_lo) & (q[i][1] == tag_hi) & ((q[i][2] & 0xffff0000u) == tag_lo) & (q[i][3] == tag_hi);
          if (__all(ok[0] & ok[1] & ok[2] & ok[3])) break;
        }
#pragma unroll
        for (int i = 0; i < 4; ++i) *(u32*)(lds + L_Q + (i >> 1) * QSTR + (i & 1) * 256 + lane * 4) = (q[i][0] & 0xffffu) | (q[i][2] << 16);
      }
      if (lane == 0) *(u64*)(lds + L_EP) = epn;
      lds_arrive(C_QRDY);
      STAMPB(4, lane);
    }
    lds_wait(C_QRDY, 1);
    if (w == 4) lds_wait(C_TURN, 8);
    STAMP(1); STAMPB(5, lane);
    bf16x8 qf[8];
#pragma unroll
    for (int j = 0; j < 8; ++j) qf[j] = *(const bf16x8*)(lds + L_Q + (lc & 1) * QSTR + lg * 16 + j * 64);
    constexpr float C2 = 0.0625f * 1.4426950408889634f;
    f32x4 acc[16];
#pragma unroll
    for (int i = 0; i < 16; ++i) acc[i] = f32x4{0.f, 0.f, 0.f, 0.f};
    auto qk = [&](int d) -> f32x4 {
      f32x4 sc = {0.f, 0.f, 0.f, 0.f};
#pragma unroll
      for (int j = 0; j < 8; ++j) sc = __builtin_amdgcn_mfma_f32_16x16x32_bf16(__builtin_bit_cast(bf16x8, kb[d][j]), qf[j], sc, 0, 0, 0);
      asm volatile("" : "+v"(sc) :: "memory"); SCHED();
      return sc;
    };
    auto pv = [&](int d, f32x4 sc) {
      const u32 sa = pk_bf16(sc[0], sc[1]), sb = pk_bf16(sc[2], sc[3]);
      const float t0 = bf2f_lo(sa), t1 = bf2f_hi(sa), t2 = bf2f_lo(sb), t3 = bf2f_hi(sb);
      const float tl = fmaxf(fmaxf(t0, t1), fmaxf(t2, t3)) * C2;
      if (__any(tl > m)) {
        const float mn = fmaxf(m, x32_max(x16_max(tl))), al = ex2(m - mn);
        m = mn; l *= al;
        const float a0 = dpp<0xA0>(al), a1 = dpp<0xF5>(al);
#pragma unroll
        for (int i = 0; i < 16; ++i) { acc[i][0] *= a0; acc[i][1] *= a1; }
      }
      const float p0 = ex2(fmaf(t0, C2, -m)), p1 = ex2(fmaf(t1, C2, -m)), p2 = ex2(fmaf(t2, C2, -m)), p3 = ex2(fmaf(t3, C2, -m));
      l += (p0 + p1) + (p2 + p3);
      const u32x2 pp = {pk_bf16(p0, p1), pk_bf16(p2, p3)};
      const s16x4 pf = __builtin_bit_cast(s16x4, pp);
#pragma unroll
      for (int hf = 0; hf < 2; ++hf)
#pragma unroll
        for (int e = 0; e < 8; ++e) {
          const u32 sel = (e & 1) ? 0x07060302u : 0x05040100u;
          const u32x2 bb = {__builtin_amdgcn_perm(vb[d][2 + hf][e >> 1], vb[d][hf][e >> 1], sel), __builtin_amdgcn_perm(vb[d][6 + hf][e >> 1], vb[d][4 + hf][e >> 1], sel)};
          acc[hf * 8 + e] = __builtin_amdgcn_mfma_f32_16x16x16bf16_1k(pf, __builtin_bit_cast(s16x4, bb), acc[hf * 8 + e], 0, 0, 0);
        }
      MEMFENCE(); SCHED();
    };
    if (w == 4) {
      u32 tA = 0, tend = 0, per = 0;
      for (u32 tp = 8; tp < NPAIR; ++tp) {
        if (PACE) {
          if (tp == KA) tA = (u32)__builtin_amdgcn_s_memrealtime();
          if (tp == KB) { const u32 tB = (u32)__builtin_amdgcn_s_memrealtime();
            if (lane == 0) __hip_atomic_store(cnt + C_MARK, (tB << 16) | (tA & 0xffffu) | 0x10000u, __ATOMIC_RELAXED, __HIP_MEMORY_SCOPE_WORKGROUP); }
          if (tp >= KC && tend == 0) {
            tend = __builtin_amdgcn_readfirstlane(__hip_atomic_load(cnt + C_TEND, __ATOMIC_RELAXED, __HIP_MEMORY_SCOPE_WORKGROUP));
            const int left = (int)(tend - (u32)__builtin_amdgcn_s_memrealtime());
            per = (tend && left > 0 && left < 4000) ? __builtin_amdgcn_readfirstlane((u32)((float)left * 128.f * __builtin_amdgcn_rcpf((float)(NPAIR - tp)))) : 0u;
          }
        }
#pragma unroll
        for (int d = 0; d < 2; ++d) {
          const u32 so = toff(2 * tp + d);
          const f32x4 sc = qk(d);
          if (PACE && per) { const u32 t = tend - (((2 * NPAIR - 2 * tp - d) * per) >> 8);
            while ((int)((u32)__builtin_amdgcn_s_memrealtime() - t) < 0) __builtin_amdgcn_s_sleep(1); SCHED(); }
          loadK(d, so); pv(d, sc); loadV(d, so);
        }
      }
    }
#pragma unroll
    for (int d = 0; d < 2; ++d) { const f32x4 sc = qk(d); pv(d, sc); }
    STAMP(2);
    l = x32_sum(x16_sum(l));
#pragma unroll
    for (int e = 0; e < 8; ++e) { const float lo = (lg & 1) ? acc[e][1] : acc[e][0], hi = (lg & 1) ? acc[8 + e][1] : acc[8 + e][0]; o[e] = (lg >> 1) ? hi : lo; }
  }
  const int ridx = (lg & 1) * 256 + (lg >> 1) * 128 + lc * 8;
  if (w != 4) {
    float* red = (float*)(lds + L_RED) + ridx;
    float* ml = (float*)(lds + L_ML);
    *(f32x4*)(red + w * 512) = f32x4{o[0], o[1], o[2], o[3]}; *(f32x4*)(red + w * 512 + 4) = f32x4{o[4], o[5], o[6], o[7]};
    if (lane < 2) *(float2*)(ml + w * 4 + lane * 2) = make_float2(m, l);
    if (lds_arrive(C_PART) == 6) {
      const int h2 = (lg & 1) * 2;
      float M = -INFINITY, L = 0.f;
      f32x4 a0 = {0.f, 0.f, 0.f, 0.f}, a1 = a0;
#pragma unroll
      for (int i = 0; i < 8; ++i) if (i != 4) M = fmaxf(M, ml[i * 4 + h2]);
#pragma unroll
      for (int i = 0; i < 8; ++i) if (i != 4) { const float e = ex2(ml[i * 4 + h2] - M);
        L = fmaf(ml[i * 4 + h2 + 1], e, L); a0 += *(const f32x4*)(red + i * 512) * e; a1 += *(const f32x4*)(red + i * 512 + 4) * e; }
      *(f32x4*)(red + 4 * 512) = a0; *(f32x4*)(red + 4 * 512 + 4) = a1;
      if (lc == 0 && lg < 2) *(float2*)(ml + 16 + h2) = make_float2(M, L);
      lds_arrive(C_MERGED);
    }
  } else {
    lds_wait(C_MERGED, 1);
    const float* red = (const float*)(lds + L_RED) + 4 * 512 + ridx;
    const f32x4 ma = *(const f32x4*)((const float*)(lds + L_ML) + 16);
    const f32x4 a0 = *(const f32x4*)red, a1 = *(const f32x4*)(red + 4);
    const float m0 = dpp<0xA0>(m), m1 = dpp<0xF5>(m), l0 = dpp<0xA0>(l), l1 = dpp<0xF5>(l);
    const float mh = (lg & 1) ? m1 : m0, lh = (lg & 1) ? l1 : l0, Ma = (lg & 1) ? ma[2] : ma[0], La = (lg & 1) ? ma[3] : ma[1];
    const float M = fmaxf(mh, Ma), e0 = ex2(mh - M), e1 = ex2(Ma - M);
    const u32 rec = WS_RA + (g * 32 + s) * RA_BLK + ((lg & 1) * 16 + (lg >> 1) * 8 + (lc >> 1)) * 80;
    const u32x2 ep2 = *(const u32x2*)(lds + L_EP);
    u32x4 v0, v1;
#pragma unroll
    for (int e = 0; e < 4; ++e) { v0[e] = __float_as_uint(fmaf(o[e], e0, a0[e] * e1)); v1[e] = __float_as_uint(fmaf(o[4 + e], e0, a1[e] * e1)); }
    const u32 Mb = __float_as_uint(M), Lb = __float_as_uint(fmaf(lh, e0, La * e1));
    STAMP(3);
    if (VAR & 8192) wait_until(DLY);
    if (!(VAR & 32)) {
    __builtin_amdgcn_raw_buffer_store_b128(v0, rW, rec + (lc & 1) * 32, 0, AUX_SC1);
    __builtin_amdgcn_raw_buffer_store_b128(v1, rW, rec + (lc & 1) * 32 + 16, 0, AUX_SC1);
    u32 ca = 0, cb = 0;
#pragma unroll
    for (int e = 0; e < 4; ++e) { chk(ca, cb, v0[e], (lc & 1) * 8 + e); chk(ca, cb, v1[e], (lc & 1) * 8 + 4 + e); }
    ca += dppu<XOR1>(ca); cb ^= dppu<XOR1>(cb);
    ca += ep2[0]; cb ^= ep2[1];
    chk(ca, cb, Mb, 16); chk(ca, cb, Lb, 17);
    __builtin_amdgcn_raw_buffer_store_b128(u32x4{Mb, Lb, ca, cb}, rW, (lc & 1) ? OOB : rec + 64, 0, AUX_SC1);
    }
    MEMFENCE();
  }

  if (PACE && w == 6) {
    u32 mk;
    while ((mk = __hip_atomic_load(cnt + C_MARK, __ATOMIC_RELAXED, __HIP_MEMORY_SCOPE_WORKGROUP)) == 0) __builtin_amdgcn_s_sleep(8);
    const u32 now = (u32)__builtin_amdgcn_s_memrealtime();
    const u32 tag = *(const u32*)(lds + L_EP);
    __builtin_amdgcn_raw_buffer_store_b64(u32x2{mk, tag}, rW, lane == 0 ? WS_PR + bid * 8 : OOB, 0, AUX_SC1);
    const u32 roff = WS_PR + (((lane >> 4) * 4 + ((bid >> 3) & 3)) * 16 + (lane & 15)) * 8;
    for (u32 k = 0; k < 4; ++k) {
      while ((u32)__builtin_amdgcn_s_memrealtime() - now < FCG + k * 150) __builtin_amdgcn_s_sleep(8);
      MEMFENCE();
      const u32x2 r = __builtin_amdgcn_raw_buffer_load_b64(rW, roff, 0, AUX_SC1);
      const bool ok = r[1] == tag;
      const float n = wave_sum(ok ? 1.f : 0.f);
      if (n < 56.f) continue;
      const float sb = wave_sum(ok ? (float)(short)((r[0] >> 16) - (mk >> 16)) : 0.f), sa = wave_sum(ok ? (float)(short)(r[0] - mk) : 0.f);
      const float rn = __builtin_amdgcn_rcpf(n), own = (float)(short)((mk >> 16) - mk);
      const float ahead = sb * rn + (NPAIR - KB) * (own + (sb - sa) * rn) * (1.f / (KB - KA)) - FCD;
      const u32 tB = now - (u32)(unsigned short)(now - (mk >> 16));
      if (lane == 0) __hip_atomic_store(cnt + C_TEND, (tB + (u32)(int)ahead) | 1u, __ATOMIC_RELAXED, __HIP_MEMORY_SCOPE_WORKGROUP);
      break;
    }
  }
  auto publish_c = [&](float y0, float y1) {
    const u32 b0 = __float_as_uint(y0), b1 = __float_as_uint(y1);
    u32 ca = 0, cb = 0;
    chk(ca, cb, b0, (lane & 7) * 2); chk(ca, cb, b1, (lane & 7) * 2 + 1);
    ca += dppu<XOR1>(ca); cb ^= dppu<XOR1>(cb); ca += dppu<XOR2>(ca); cb ^= dppu<XOR2>(cb); ca += dppu<XOR7>(ca); cb ^= dppu<XOR7>(cb);
    ca += (u32)epn; cb ^= (u32)(epn >> 32);
    const u32 z0 = dppu<XOR1>(b0), z1 = dppu<XOR1>(b1);
    const u32 rec = WS_RC + (w * 7 + (lane >> 3)) * RC_BLK + cslot * 128;
    const bool odd = lane & 1;
    const u32x4 ov = {odd ? ca : b0, odd ? cb : b1, odd ? ca : z0, odd ? cb : z1};
    __builtin_amdgcn_raw_buffer_store_b128(ov, rW, lane >= 56 ? OOB : !odd ? rec + (lane & 7) * 8 : (lane & 7) == 1 ? rec + 64 : OOB, 0, AUX_SC1);
    MEMFENCE();
  };
  auto gather_c = [&](int slot0, int nslot, u32 e0, u32 e1) {
    const u32 off = WS_RC + fid * RC_BLK + (slot0 + (lane >> 2)) * 128;
    u32x4 yv = {0u, 0u, 0u, 0u}; u32x2 cv;
    bool ok = (lane >> 2) >= nslot;
    for (u32 spins = 0; spins < SPIN_CAP; ++spins) {
      MEMFENCE();
      if (!ok) {
        yv = __builtin_amdgcn_raw_buffer_load_b128(rW, off + (lane & 3) * 16, 0, AUX_SC1);
        cv = __builtin_amdgcn_raw_buffer_load_b64(rW, off + 64, 0, AUX_SC1);
        u32 ca = 0, cb = 0;
#pragma unroll
        for (int e = 0; e < 4; ++e) chk(ca, cb, yv[e], (lane & 3) * 4 + e);
        ca += dppu<XOR1>(ca); cb ^= dppu<XOR1>(cb); ca += dppu<XOR2>(ca); cb ^= dppu<XOR2>(cb);
        u32 bad = ((ca + e0) ^ cv[0]) | ((cb ^ e1) ^ cv[1]);
        bad |= dppu<XOR1>(bad); bad |= dppu<XOR2>(bad);
        ok = bad == 0;
      }
      const int miss = __popcll(__ballot(!ok));
      if (miss == 0) break;
      for (int i = 8; i < miss; i += 8) __builtin_amdgcn_s_sleep(CSLEEP);
    }
    const float v0 = t32_sum(__uint_as_float(yv[0]), __uint_as_float(yv[2])), v1 = t32_sum(__uint_as_float(yv[1]), __uint_as_float(yv[3]));
    float t = t16_sum(v0, v1);
    t += dpp<ROR4>(t); t += dpp<XOR8>(t);
    if (!(lane & 12)) ((float*)(lds + L_FIN))[(w ^ 4) * 16 + (lane & 3) * 4 + lg] = t;
  };

  u32 wp[64];
  if (w < 4 && (has_mlp || cons)) {
    const u32 rowoff = lane < 56 ? lane * 16 : OOB;
    const int l2 = w * 56 + (lane < 56 ? lane : 0);
    if (CHT) wait_until(1200 + (((s - 16) & ~1) * 4 + (cons ? (s & 1) * 4 : 0) + w) * CHT);
    if (has_mlp) {
      const auto rW1 = __builtin_amdgcn_make_buffer_rsrc((void*)p.w1, 0, (ABL & 1) ? 0 : D * FF * 2, 0x00020000);
      const auto rW2 = __builtin_amdgcn_make_buffer_rsrc((void*)p.w2, 0, (ABL & 2) ? 0 : D * FF * 2, 0x00020000);
      const auto rB1 = __builtin_amdgcn_make_buffer_rsrc((void*)p.b1, 0, FF * 2, 0x00020000);
      const int u0 = mi * 32;
      u32x4 w1r[7], w2r[8];
#pragma unroll
      for (int i = 0; i < 7; ++i) w1r[i] = __builtin_amdgcn_raw_buffer_load_b128(rW1, (w * 16 + (lane >> 2)) * (FF * 2) + u0 * 2 + (lane & 3) * 16, i * 64 * (FF * 2), AUX_NT);
#pragma unroll
      for (int i = 0; i < 8; ++i) w2r[i] = __builtin_amdgcn_raw_buffer_load_b128(rW2, rowoff, (u0 + w * 8 + i) * 896, AUX_NT);
      const u32 b1r = __builtin_amdgcn_raw_buffer_load_b16(rB1, (u0 + w * 8 + (lane & 7)) * 2, 0, 0);
      gemv32(w1r);
      lds_arrive(C_HP);
      lds_wait(C_HP, 4);
      const float* hp = (const float*)(lds + L_QP) + w * 8 + (lane & 7);
      float hv = bfr(bfr((hp[0] + hp[32]) + (hp[64] + hp[96])) + bf2f_lo(b1r));
      hv = bfr(0.5f * hv * (1.f + tanhf(0.7978845608028654f * (hv + 0.044715f * hv * hv * hv))));
      float ym[8];
#pragma unroll
      for (int e = 0; e < 8; ++e) ym[e] = 0.f;
#pragma unroll
      for (int r = 0; r < 8; ++r) {
        const float hr = __builtin_bit_cast(float, __builtin_amdgcn_readlane(__builtin_bit_cast(int, hv), r));
#pragma unroll
        for (int e = 0; e < 4; ++e) { ym[2 * e] = fmaf(hr, bf2f_lo(w2r[r][e]), ym[2 * e]); ym[2 * e + 1] = fmaf(hr, bf2f_hi(w2r[r][e]), ym[2 * e + 1]); }
      }
      if (lane < 56) { float* y = (float*)(lds + L_YM) + w * 448 + lane * 8;
        *(f32x4*)y = f32x4{ym[0], ym[1], ym[2], ym[3]}; *(f32x4*)(y + 4) = f32x4{ym[4], ym[5], ym[6], ym[7]}; }
      lds_arrive(C_CHORE);
      lds_wait(C_CHORE, 4);
      const float2* y = (const float2*)(lds + L_YM) + l2;
      const float2 a = y[0], b = y[224], c = y[448], d = y[672];
      if (!(VAR & 64)) publish_c((a.x + b.x) + (c.x + d.x), (a.y + b.y) + (c.y + d.y));
    } else {
      const auto rWo = __builtin_amdgcn_make_buffer_rsrc((void*)p.wo, 0, (ABL & 4) ? 0 : D * HQ * DH * 2, 0x00020000);
      u32x4 wor[16];
#pragma unroll
      for (int i = 0; i < 16; ++i) wor[i] = __builtin_amdgcn_raw_buffer_load_b128(rWo, rowoff, (g * 512 + (s - 24) * 64 + w * 16 + i) * 896, AUX_NT);
      if (lane < 56) {
#pragma unroll
        for (int i = 0; i < 16; ++i) *(u32x4*)(lds + L_WO + (w * 16 + i) * 896 + lane * 16) = wor[i];
      }
      lds_arrive(C_CHORE);
      if (fin) {
        if (tid < 16) { const int col = fid * 16 + tid;
          ((float*)(lds + L_XB))[tid] = __bfloat162float(((const __hip_bfloat16*)p.x)[col]) + __bfloat162float(((const __hip_bfloat16*)p.b2)[col]); }
        wait_until(1200 + 64 * CHT + 400);
        if (!(VAR & (32 | 2048))) gather_c(64 + w * 16, w < 3 ? 16 : 8, (u32)epn, (u32)(epn >> 32));
      }
      lds_wait(C_CHORE, 4);
#pragma unroll
      for (int k = 0; k < 32; ++k) {
        const u32 a = ((const u32*)(lds + L_WO))[(2 * k) * 224 + l2], b = ((const u32*)(lds + L_WO))[(2 * k + 1) * 224 + l2];
        wp[2 * k] = __builtin_amdgcn_perm(b, a, 0x05040100u); wp[2 * k + 1] = __builtin_amdgcn_perm(b, a, 0x07060302u);
      }
      STAMPB(6, wp[63]);
    }
  }
  if (cons && !(VAR & (32 | 2048))) {
    bar();
    const u32x2 ep2 = *(const u32x2*)(lds + L_EP);
    if (w >= 4) {
      const u32 roff = WS_RA + (g * 32 + (lane >> 2)) * RA_BLK + ((s - 24) * 4 + w - 4) * 80;
      u32x4 ov[2], mv[2];
      bool ok[2] = {false, false};
      for (u32 spins = 0; spins < SPIN_CAP; ++spins) {
        MEMFENCE();
#pragma unroll
        for (int i = 0; i < 2; ++i) if (!ok[i]) {
          ov[i] = __builtin_amdgcn_raw_buffer_load_b128(rW, roff + i * 16 * RA_BLK + (lane & 3) * 16, 0, AUX_SC1);
          mv[i] = __builtin_amdgcn_raw_buffer_load_b128(rW, roff + i * 16 * RA_BLK + 64, 0, AUX_SC1);
        }
        int miss = 0;
#pragma unroll
        for (int i = 0; i < 2; ++i) {
          u32 ca = 0, cb = 0;
#pragma unroll
          for (int e = 0; e < 4; ++e) chk(ca, cb, ov[i][e], (lane & 3) * 4 + e);
          ca += dppu<XOR1>(ca); cb ^= dppu<XOR1>(cb); ca += dppu<XOR2>(ca); cb ^= dppu<XOR2>(cb);
          ca += ep2[0]; cb ^= ep2[1];
          chk(ca, cb, mv[i][0], 16); chk(ca, cb, mv[i][1], 17);
          u32 bad = (ca ^ mv[i][2]) | (cb ^ mv[i][3]);
          bad |= dppu<XOR1>(bad); bad |= dppu<XOR2>(bad);
          ok[i] = bad == 0;
          miss += __popcll(__ballot(!ok[i]));
        }
        if (miss == 0) break;
        for (int i = PTHR; i < miss; i += 4) __builtin_amdgcn_s_sleep(PSLEEP);
      }
      const float m0 = __uint_as_float(mv[0][0]), m1 = __uint_as_float(mv[1][0]);
      float M = fmaxf(m0, m1);
      M = fmaxf(M, dpp<ROR4>(M)); M = fmaxf(M, dpp<XOR8>(M)); M = x32_max(x16_max(M));
      const float w0 = ex2(m0 - M), w1 = ex2(m1 - M);
      float L = fmaf(w0, __uint_as_float(mv[0][1]), w1 * __uint_as_float(mv[1][1]));
      L += dpp<ROR4>(L); L += dpp<XOR8>(L); L = x32_sum(x16_sum(L));
      float oo[4];
#pragma unroll
      for (int e = 0; e < 4; ++e) oo[e] = fmaf(w0, __uint_as_float(ov[0][e]), w1 * __uint_as_float(ov[1][e]));
      oo[0] = t32_sum(oo[0], oo[2]); oo[1] = t32_sum(oo[1], oo[3]);
      float t = t16_sum(oo[0], oo[1]);
      t += dpp<ROR4>(t); t += dpp<XOR8>(t);
      if (!(lane & 12)) ((unsigned short*)(lds + L_O))[(w - 4) * 16 + (lane & 3) * 4 + lg] = (unsigned short)pk_bf16(t * __builtin_amdgcn_rcpf(L), 0.f);
    }
    bar();
    STAMP(5);
    if (w < 4) {
      float y0 = 0.f, y1 = 0.f, y2 = 0.f, y3 = 0.f;
#pragma unroll
      for (int k4 = 0; k4 < 8; ++k4) { const u32x4 op = ((const u32x4*)(lds + L_O))[k4];
#pragma unroll
        for (int e = 0; e < 4; e += 2) {
          y0 = dot2(wp[8 * k4 + 2 * e], op[e], y0); y1 = dot2(wp[8 * k4 + 2 * e + 1], op[e], y1);
          y2 = dot2(wp[8 * k4 + 2 * e + 2], op[e + 1], y2); y3 = dot2(wp[8 * k4 + 2 * e + 3], op[e + 1], y3);
        } }
      publish_c(y0 + y2, y1 + y3);
    }
    STAMP(6);
    if (fin) {
      if (w >= 4) gather_c((w - 4) * 16, 16, ep2[0], ep2[1]);
      bar();
      if (tid < 16) {
        const float* f = (const float*)(lds + L_FIN) + tid;
        const float r = (((const float*)(lds + L_XB))[tid] + ((f[64] + f[80]) + (f[96] + f[112]))) + ((f[0] + f[16]) + (f[32] + f[48]));
        ((unsigned short*)p.out)[fid * 16 + tid] = (unsigned short)pk_bf16(r, 0.f);
        if (fid == 0 && tid == 0) __builtin_amdgcn_raw_buffer_store_b64(ep2, rW, WS_EPOCH, 0, AUX_SC1);
      }
    }
  }
  if ((VAR & (32 | 2048)) && cons && w < 4 && wp[5] == 0x12345u) ((float*)p.ws)[77] = 1.f;
}

extern "C" void kernel_launch(void* const* d_in, const int* in_sizes, int n_in, void* d_out, int out_size, void* d_ws, size_t ws_size, hipStream_t stream) {
  Params p;
  p.x = d_in[0]; p.k = d_in[1]; p.v = d_in[2]; p.wq = d_in[3]; p.wo = d_in[4];
  p.w1 = d_in[6]; p.b1 = d_in[7]; p.w2 = d_in[8]; p.b2 = d_in[9];
  p.out = d_out; p.ws = d_ws;
  hipLaunchKernelGGL(fused_decode, dim3(NWG), dim3(512), 0, stream, p);
}
```

```cpp
#define DT 1
#define DA 0u
#define DB 134217728u
#include <hip/hip_runtime.h>
#include <hip/hip_bf16.h>
#include <stdint.h>

typedef unsigned u32;
typedef unsigned long long u64;
typedef u32 u32x4 __attribute__((ext_vector_type(4)));
typedef u32 u32x2 __attribute__((ext_vector_type(2)));
typedef float f32x4 __attribute__((ext_vector_type(4)));
typedef __bf16 bf16x8 __attribute__((ext_vector_type(8)));
typedef __bf16 bf16x2 __attribute__((ext_vector_type(2)));
typedef short s16x4 __attribute__((ext_vector_type(4)));
#define DI __device__ __forceinline__

#define STAMPS 0
#define TMASK 0x13a
#define QT 400
#define KVT 0
#define CHT 20
#define PSLEEP 2
#define ABL 0
#define VAR 0
#define PRET 2
#define DLY 7900
#define CSLEEP 6
#define PTHR 8
#define HEADMAP 1
#define HPRIO 3

constexpr int D = 448, HQ = 16, SK = 65536, DH = 256, FF = 1792;
constexpr int NWG = 256, NWAVE_TOTAL = 8;
constexpr int NPAIR = 64;
constexpr int NFIN = 28;
constexpr u32 WS_EPOCH = 0;
constexpr u32 WS_QG = 4096;
constexpr u32 WS_RA = WS_QG + 8 * 512 * 8;
constexpr u32 RA_BLK = 32 * 80;
constexpr u32 WS_RC = WS_RA + NWG * RA_BLK;
constexpr u32 RC_BLK = 128 * 128;
constexpr u32 WS_END = WS_RC + NFIN * RC_BLK;
constexpr u32 WS_STAMP = 2u << 20;
static_assert(WS_END <= WS_STAMP, "");
constexpr int AUX_NT = 2, AUX_SC1 = 16;
constexpr u32 SPIN_CAP = 1u << 15;
constexpr u32 OOB = 0x7fffff00u;

#define MEMFENCE() asm volatile("" ::: "memory")
#define SCHED() __builtin_amdgcn_sched_barrier(0)

DI float bf2f_lo(u32 x) { return __builtin_bit_cast(float, x << 16); }
DI float bf2f_hi(u32 x) { return __builtin_bit_cast(float, x & 0xffff0000u); }
typedef float f32x2 __attribute__((ext_vector_type(2)));
DI u32 pk_bf16(float a, float b) { const f32x2 f = {a, b}; return __builtin_bit_cast(u32, __builtin_convertvector(f, bf16x2)); }
DI float bfr(float a) { return bf2f_lo(pk_bf16(a, 0.f)); }
template <int CTRL> DI float dpp(float x) {
  return __builtin_bit_cast(float, __builtin_amdgcn_mov_dpp(__builtin_bit_cast(int, x), CTRL, 0xf, 0xf, true));
}
constexpr int XOR1 = 0xB1, XOR2 = 0x4E, XOR3 = 0x1B, XOR7 = 0x141, XOR8 = 0x128, ROR4 = 0x124;
DI float t32_sum(float u, float v) { auto s = __builtin_amdgcn_permlane32_swap(__float_as_uint(u), __float_as_uint(v), false, false); return __uint_as_float(s[0]) + __uint_as_float(s[1]); }
DI float t16_sum(float u, float v) { auto s = __builtin_amdgcn_permlane16_swap(__float_as_uint(u), __float_as_uint(v), false, false); return __uint_as_float(s[0]) + __uint_as_float(s[1]); }
DI float x16_sum(float x) { return t16_sum(x, x); }
DI float x32_sum(float x) { return t32_sum(x, x); }
DI float x16_max(float x) { auto s = __builtin_amdgcn_permlane16_swap(__float_as_uint(x), __float_as_uint(x), false, false); return fmaxf(__uint_as_float(s[0]), __uint_as_float(s[1])); }
DI float x32_max(float x) { auto s = __builtin_amdgcn_permlane32_swap(__float_as_uint(x), __float_as_uint(x), false, false); return fmaxf(__uint_as_float(s[0]), __uint_as_float(s[1])); }
DI float row_sum(float x) { x += dpp<XOR1>(x); x += dpp<XOR2>(x); x += dpp<XOR7>(x); x += dpp<XOR8>(x); return x; }
DI float wave_sum(float x) { return x32_sum(x16_sum(row_sum(x))); }
template <int CTRL> DI u32 dppu(u32 x) { return (u32)__builtin_amdgcn_mov_dpp((int)x, CTRL, 0xf, 0xf, true); }
DI u32 rotl(u32 x, u32 r) { return __builtin_amdgcn_alignbit(x, x, 32u - r); }
DI void chk(u32& ca, u32& cb, u32 v, u32 i) { ca += v; cb ^= rotl(v, i + 1); }
DI void bar() { asm volatile("s_waitcnt lgkmcnt(0)\n\ts_barrier" ::: "memory"); }
DI float dot2(u32 a, u32 b, float c) { return __builtin_amdgcn_fdot2_f32_bf16(__builtin_bit_cast(bf16x2, a), __builtin_bit_cast(bf16x2, b), c, false); }
DI float ex2(float x) { return __builtin_amdgcn_exp2f(x); }
template <u32 M> DI u32 pdep(u32 x) {
  u32 r = 0, k = 0;
#pragma unroll
  for (int b = 0; b < 12; ++b) if ((M >> b) & 1) { r |= ((x >> k) & 1u) << b; ++k; }
  return r;
}

struct Params {
  const void* x; const void* k; const void* v; const void* wq; const void* wo;
  const void* w1; const void* b1; const void* w2; const void* b2;
  void* out; void* ws;
};

constexpr int L_XN = 0;
constexpr int QSTR = 576;
constexpr int L_Q = L_XN + 2048;
constexpr int L_WO = L_Q + 2 * QSTR;
constexpr int L_QP = L_WO + 64 * 896;
constexpr int L_YM = L_QP + 512;
constexpr int L_RED = L_YM + 4 * 448 * 4;
constexpr int L_ML = L_RED + 8 * 2048;
constexpr int L_O = L_ML + 128;
constexpr int L_FIN = L_O + 128;
constexpr int L_XB = L_FIN + 512;
constexpr int L_EP = L_XB + 64;
constexpr int L_CNT = L_EP + 16;
constexpr int L_END = L_CNT + 64;
enum { C_QPART, C_QRDY, C_HP, C_CHORE, C_TURN, C_PART, C_MERGED };

__global__ __launch_bounds__(512, 2) void fused_decode(Params p) {
  __shared__ __attribute__((aligned(16))) char lds[L_END];
  const int tid = threadIdx.x, lane = tid & 63;
  const int w = __builtin_amdgcn_readfirstlane(tid >> 6);
  const int bid = blockIdx.x;
  const int g = HEADMAP == 0 ? bid & 7 : HEADMAP == 1 ? ((bid >> 6) << 1) | ((bid >> 2) & 1) : bid >> 5;
  const int s = HEADMAP == 0 ? bid >> 3 : HEADMAP == 1 ? (((bid >> 4) & 3) << 3) | ((bid & 3) << 1) | ((bid >> 3) & 1) : bid & 31;
  const int lg = lane >> 4, lc = lane & 15;
#define STAMPC(i)
#define STAMP(i)
#define STAMPB(i, v)
  STAMP(0);
  const auto rW = __builtin_amdgcn_make_buffer_rsrc(p.ws, 0, WS_END, 0x00020000);
  u64 epn = 0;
  u32* const cnt = (u32*)(lds + L_CNT);
  float* const xn = (float*)(lds + L_XN);
  auto lds_arrive = [&](int i) -> u32 {
    asm volatile("s_waitcnt lgkmcnt(0)" ::: "memory");
    u32 r = 0;
    if (lane == 0) r = __hip_atomic_fetch_add(cnt + i, 1u, __ATOMIC_RELAXED, __HIP_MEMORY_SCOPE_WORKGROUP);
    r = __builtin_amdgcn_readfirstlane(r);
    MEMFENCE();
    return r;
  };
  auto lds_wait = [&](int i, u32 v) { while (__hip_atomic_load(cnt + i, __ATOMIC_RELAXED, __HIP_MEMORY_SCOPE_WORKGROUP) != v) __builtin_amdgcn_s_sleep(1); MEMFENCE(); };
  const u64 tstart0 = __builtin_amdgcn_s_memrealtime();
  u64 tstart = tstart0;
  auto wait_until = [&](u32 ticks) { while ((u32)(__builtin_amdgcn_s_memrealtime() - tstart) < ticks) __builtin_amdgcn_s_sleep(1); };
  const bool q_prod = s < 16, has_mlp = s >= 16 && s < (g < 4 ? 24 : 22), cons = s >= 24;
  const int mi = (g < 4 ? g * 8 : 8 + g * 6) + s - 16;
  const int cslot = cons ? (s - 24) * 8 + g : 64 + mi;
  const int fid = (s - 24) * 8 + g;
  const bool fin = cons && fid < NFIN;
  auto gemv32 = [&](const u32x4 (&wr)[7]) {
    float a[8];
#pragma unroll
    for (int e = 0; e < 8; ++e) a[e] = 0.f;
#pragma unroll
    for (int i = 0; i < 7; ++i) { const float xx = xn[16 * (w + 4 * i) + (lane >> 2)];
#pragma unroll
      for (int e = 0; e < 4; ++e) { a[2 * e] = fmaf(xx, bf2f_lo(wr[i][e]), a[2 * e]); a[2 * e + 1] = fmaf(xx, bf2f_hi(wr[i][e]), a[2 * e + 1]); } }
#pragma unroll
    for (int e = 0; e < 4; ++e) a[e] = t32_sum(a[e], a[e + 4]);
#pragma unroll
    for (int e = 0; e < 2; ++e) a[e] = t16_sum(a[e], a[e + 2]);
    const float t1 = a[0] + dpp<XOR8>(a[0]), t2 = a[1] + dpp<XOR8>(a[1]);
    float t = (lane & 8) ? t2 : t1;
    t += dpp<XOR7>(dpp<XOR3>(t));
    if (!(lane & 4)) ((float*)(lds + L_QP))[w * 32 + (lane & 3) * 8 + (lane >> 3)] = t;
  };
  u32 diag_c = 0;
  if (bid == 0 && w == 7) {
    const auto rD = __builtin_amdgcn_make_buffer_rsrc((void*)(DT == 0 ? p.k : DT == 1 ? p.v : DT == 2 ? p.wq : p.ws), 0, 0x40000000, 0x00020000);
    u32 x = 12345u;
    for (int i = 0; i < 32; ++i) {
      x = x * 1664525u + 1013904223u; u32 h = x ^ (x >> 15); h *= 0x2c1b3c6du; h ^= h >> 13;
      const u32 off = (DA + (u32)(((u64)h * (DB - DA)) >> 32)) & ~127u;
      const u64 t0 = __builtin_amdgcn_s_memtime();
      asm volatile("s_waitcnt lgkmcnt(0)" ::: "memory");
      const u32 v = __builtin_amdgcn_raw_buffer_load_b32(rD, 0, off, 2);
      asm volatile("s_waitcnt vmcnt(0)" :: "v"(v) : "memory");
      const u64 t1 = __builtin_amdgcn_s_memtime();
      asm volatile("s_waitcnt lgkmcnt(0)" ::: "memory");
      diag_c += (u32)(t1 - t0) < 600u ? 1u : 0u;
    }
  }
  while ((u32)(__builtin_amdgcn_s_memrealtime() - tstart0) < 4000) __builtin_amdgcn_s_sleep(4);
  tstart = __builtin_amdgcn_s_memrealtime();
  if (PRET && w < 4) {
    const u32x4 z = {0u, 0u, 0u, 0u};
    __builtin_amdgcn_raw_buffer_store_b128(z, rW, tid < 160 ? WS_RA + (g * 32 + s) * RA_BLK + tid * 16 : OOB, 0, AUX_SC1);
    if (PRET > 1 && (cons || has_mlp)) __builtin_amdgcn_raw_buffer_store_b128(z, rW, lane < 56 ? WS_RC + (w * 7 + (lane >> 3)) * RC_BLK + cslot * 128 + (lane & 7) * 16 : OOB, 0, AUX_SC1);
    MEMFENCE();
  }
  u32x4 xr, wq[7];
  u32x2 ep = {0u, 0u};
  if (w < 4 || w == 5) ep = __builtin_amdgcn_raw_buffer_load_b64(rW, WS_EPOCH, 0, AUX_SC1);
  u32 tag_lo = 0, tag_hi = 0;
  auto take_epoch = [&]() {
    asm volatile("" : "+v"(ep));
    const u64 ep64 = ((u64)(u32)__builtin_amdgcn_readfirstlane(ep[1]) << 32) | (u32)__builtin_amdgcn_readfirstlane(ep[0]);
    epn = ep64 * 6364136223846793005ull + 1442695040888963407ull;
    tag_lo = (u32)epn << 16; tag_hi = (u32)(epn >> 16);
  };
  if (w < 4) {
    __builtin_amdgcn_s_setprio(HPRIO);
    const auto rX = __builtin_amdgcn_make_buffer_rsrc((void*)p.x, 0, D * 2, 0x00020000);
    const auto rWq = __builtin_amdgcn_make_buffer_rsrc((void*)p.wq, 0, ((ABL & 8) || !q_prod) ? 0 : D * HQ * DH * 2, 0x00020000);
    xr = __builtin_amdgcn_raw_buffer_load_b128(rX, lane * 16, 0, 0);
    const u32 wqo = (w * 16 + (lane >> 2)) * 8192 + (g * 512 + s * 32) * 2 + (lane & 3) * 16;
#pragma unroll
    for (int i = 0; i < 7; ++i) wq[i] = __builtin_amdgcn_raw_buffer_load_b128(rWq, wqo, i * 64 * 8192, AUX_NT);
  }
  if (w != 4) {
    if (tid < 16) cnt[tid] = 0;
    bar();
  }
  if (__builtin_expect(w < 4, 0)) {
    {
      float xv[8];
#pragma unroll
      for (int e = 0; e < 4; ++e) { xv[2 * e] = bf2f_lo(xr[e]); xv[2 * e + 1] = bf2f_hi(xr[e]); }
      STAMPB(1, xv[0]);
      const float mean = wave_sum(((xv[0] + xv[1]) + (xv[2] + xv[3])) + ((xv[4] + xv[5]) + (xv[6] + xv[7]))) * (1.f / D);
      float sq = 0.f;
#pragma unroll
      for (int e = 0; e < 8; ++e) { const float d = xv[e] - mean; sq = fmaf(d, d, sq); }
      sq = lane < 56 ? sq : 0.f;
      const float var = wave_sum(sq) * (1.f / D);
      const float mb = bfr(mean), den = bfr(__builtin_amdgcn_sqrtf(bfr(bfr(var) + 1e-5f)));
      const float rden = lane < 56 ? __builtin_amdgcn_rcpf(den) : 0.f;
      f32x4 o[2];
#pragma unroll
      for (int e = 0; e < 8; ++e) o[e >> 2][e & 3] = bfr(bfr(xv[e] - mb) * rden);
      *(f32x4*)(xn + lane * 8) = o[0]; *(f32x4*)(xn + lane * 8 + 4) = o[1];
    }
    take_epoch();
    if (q_prod) {
      gemv32(wq);
      STAMPB(2, lane);
      if (lds_arrive(C_QPART) == 3) {
        const float* qp = (const float*)(lds + L_QP) + (lane & 31);
        const float q = (qp[0] + qp[32]) + (qp[64] + qp[96]);
        const u32x2 gr = {tag_lo | (pk_bf16(q, 0.f) & 0xffffu), tag_hi};
        __builtin_amdgcn_raw_buffer_store_b64(gr, rW, lane < 32 ? WS_QG + (g * 512 + s * 32 + lane) * 8 : OOB, 0, AUX_SC1);
      }
    }
    MEMFENCE();
    STAMPB(3, lane);
    __builtin_amdgcn_s_setprio(0);
  }

  float m = -INFINITY, l = 0.f, o[8];
  {
    const auto rK = __builtin_amdgcn_make_buffer_rsrc((void*)p.k, 0, 0x10000000, 0x00020000);
    const auto rV = __builtin_amdgcn_make_buffer_rsrc((void*)p.v, 0, 0x10000000, 0x00020000);
    u32x4 kb[2][8], vb[2][8];
    const u32 koff = (4 * (lc & 3) + (lc >> 2)) * 512 + lg * 16;
    const u32 voff = lg * 512 + lc * 16;
    const u32 tbase = g * (u32)(SK * 512) + (pdep<TMASK>(s) << 13);
    auto toff = [&](u32 i) -> u32 { return tbase + (pdep<~TMASK & 0xfff>(i) << 13); };
    auto loadK = [&](int d, u32 so) {
      u32 oo = koff; asm volatile("" : "+v"(oo));
#pragma unroll
      for (int j = 0; j < 8; ++j) kb[d][j] = __builtin_amdgcn_raw_buffer_load_b128(rK, oo + j * 64, so, AUX_NT);
      MEMFENCE(); SCHED();
    };
    auto loadV = [&](int d, u32 so) {
      u32 oo = voff; asm volatile("" : "+v"(oo));
#pragma unroll
      for (int j = 0; j < 8; ++j) vb[d][j] = __builtin_amdgcn_raw_buffer_load_b128(rV, oo + ((j >> 1) & 1) * 2048 + (j & 1) * 256, so + (j >> 2) * 4096, AUX_NT);
      MEMFENCE(); SCHED();
    };
    const u32 pair0 = w ^ 4;
    {
      const u32 so0 = toff(2 * pair0), so1 = toff(2 * pair0 + 1);
      asm volatile("" :: "s"(so0), "s"(so1), "v"(koff), "v"(voff));
      if (pair0) lds_wait(C_TURN, pair0); else if (KVT && q_prod) wait_until(KVT);
      STAMPC(1);
      if (w >= 4) STAMPB(2, lane);
      loadK(0, so0);
      if (w == 4) bar();
      loadV(0, so0); loadK(1, so1); loadV(1, so1);
      if (lane == 0) __hip_atomic_store(cnt + C_TURN, pair0 + 1, __ATOMIC_RELAXED, __HIP_MEMORY_SCOPE_WORKGROUP);
      MEMFENCE();
    }
    STAMPC(2);
    if (w >= 4) STAMPB(1, lane);
    if (w == 5) {
      take_epoch();
      if (!(VAR & 8)) {
        const u32 qgoff = WS_QG + (g * 512 + lane * 2) * 8;
        u32x4 q[4];
        bool ok[4] = {false, false, false, false};
        wait_until(QT);
        for (u32 spins = 0; spins < SPIN_CAP; ++spins) {
          MEMFENCE();
#pragma unroll
          for (int i = 0; i < 4; ++i) if (!ok[i]) q[i] = __builtin_amdgcn_raw_buffer_load_b128(rW, qgoff + i * 1024, 0, AUX_SC1);
#pragma unroll
          for (int i = 0; i < 4; ++i) ok[i] = ((q[i][0] & 0xffff0000u) == tag_lo) & (q[i][1] == tag_hi) & ((q[i][2] & 0xffff0000u) == tag_lo) & (q[i][3] == tag_hi);
          if (__all(ok[0] & ok[1] & ok[2] & ok[3])) break;
        }
#pragma unroll
        for (int i = 0; i < 4; ++i) *(u32*)(lds + L_Q + (i >> 1) * QSTR + (i & 1) * 256 + lane * 4) = (q[i][0] & 0xffffu) | (q[i][2] << 16);
      }
      if (lane == 0) *(u64*)(lds + L_EP) = epn;
      lds_arrive(C_QRDY);
      STAMPB(4, lane);
    }
    lds_wait(C_QRDY, 1);
    if (w == 4) lds_wait(C_TURN, 8);
    STAMP(1); STAMPB(5, lane);
    bf16x8 qf[8];
#pragma unroll
    for (int j = 0; j < 8; ++j) qf[j] = *(const bf16x8*)(lds + L_Q + (lc & 1) * QSTR + lg * 16 + j * 64);
    constexpr float C2 = 0.0625f * 1.4426950408889634f;
    f32x4 acc[16];
#pragma unroll
    for (int i = 0; i < 16; ++i) acc[i] = f32x4{0.f, 0.f, 0.f, 0.f};
    auto qk = [&](int d) -> f32x4 {
      f32x4 sc = {0.f, 0.f, 0.f, 0.f};
#pragma unroll
      for (int j = 0; j < 8; ++j) sc = __builtin_amdgcn_mfma_f32_16x16x32_bf16(__builtin_bit_cast(bf16x8, kb[d][j]), qf[j], sc, 0, 0, 0);
      asm volatile("" : "+v"(sc) :: "memory"); SCHED();
      return sc;
    };
    auto pv = [&](int d, f32x4 sc) {
      const u32 sa = pk_bf16(sc[0], sc[1]), sb = pk_bf16(sc[2], sc[3]);
      const float t0 = bf2f_lo(sa), t1 = bf2f_hi(sa), t2 = bf2f_lo(sb), t3 = bf2f_hi(sb);
      const float tl = fmaxf(fmaxf(t0, t1), fmaxf(t2, t3)) * C2;
      if (__any(tl > m)) {
        const float mn = fmaxf(m, x32_max(x16_max(tl))), al = ex2(m - mn);
        m = mn; l *= al;
        const float a0 = dpp<0xA0>(al), a1 = dpp<0xF5>(al);
#pragma unroll
        for (int i = 0; i < 16; ++i) { acc[i][0] *= a0; acc[i][1] *= a1; }
      }
      const float p0 = ex2(fmaf(t0, C2, -m)), p1 = ex2(fmaf(t1, C2, -m)), p2 = ex2(fmaf(t2, C2, -m)), p3 = ex2(fmaf(t3, C2, -m));
      l += (p0 + p1) + (p2 + p3);
      const u32x2 pp = {pk_bf16(p0, p1), pk_bf16(p2, p3)};
      const s16x4 pf = __builtin_bit_cast(s16x4, pp);
#pragma unroll
      for (int hf = 0; hf < 2; ++hf)
#pragma unroll
        for (int e = 0; e < 8; ++e) {
          const u32 sel = (e & 1) ? 0x07060302u : 0x05040100u;
          const u32x2 bb = {__builtin_amdgcn_perm(vb[d][2 + hf][e >> 1], vb[d][hf][e >> 1], sel), __builtin_amdgcn_perm(vb[d][6 + hf][e >> 1], vb[d][4 + hf][e >> 1], sel)};
          acc[hf * 8 + e] = __builtin_amdgcn_mfma_f32_16x16x16bf16_1k(pf, __builtin_bit_cast(s16x4, bb), acc[hf * 8 + e], 0, 0, 0);
        }
      MEMFENCE(); SCHED();
    };
    if (w == 4) {
      for (u32 tp = 8; tp < NPAIR; ++tp) {
#pragma unroll
        for (int d = 0; d < 2; ++d) {
          const u32 so = toff(2 * tp + d);
          const f32x4 sc = qk(d);
          loadK(d, so); pv(d, sc); loadV(d, so);
        }
      }
    }
#pragma unroll
    for (int d = 0; d < 2; ++d) { const f32x4 sc = qk(d); pv(d, sc); }
    STAMP(2);
    l = x32_sum(x16_sum(l));
#pragma unroll
    for (int e = 0; e < 8; ++e) { const float lo = (lg & 1) ? acc[e][1] : acc[e][0], hi = (lg & 1) ? acc[8 + e][1] : acc[8 + e][0]; o[e] = (lg >> 1) ? hi : lo; }
  }
  const int ridx = (lg & 1) * 256 + (lg >> 1) * 128 + lc * 8;
  if (w != 4) {
    float* red = (float*)(lds + L_RED) + ridx;
    float* ml = (float*)(lds + L_ML);
    *(f32x4*)(red + w * 512) = f32x4{o[0], o[1], o[2], o[3]}; *(f32x4*)(red + w * 512 + 4) = f32x4{o[4], o[5], o[6], o[7]};
    if (lane < 2) *(float2*)(ml + w * 4 + lane * 2) = make_float2(m, l);
    if (lds_arrive(C_PART) == 6) {
      const int h2 = (lg & 1) * 2;
      float M = -INFINITY, L = 0.f;
      f32x4 a0 = {0.f, 0.f, 0.f, 0.f}, a1 = a0;
#pragma unroll
      for (int i = 0; i < 8; ++i) if (i != 4) M = fmaxf(M, ml[i * 4 + h2]);
#pragma unroll
      for (int i = 0; i < 8; ++i) if (i != 4) { const float e = ex2(ml[i * 4 + h2] - M);
        L = fmaf(ml[i * 4 + h2 + 1], e, L); a0 += *(const f32x4*)(red + i * 512) * e; a1 += *(const f32x4*)(red + i * 512 + 4) * e; }
      *(f32x4*)(red + 4 * 512) = a0; *(f32x4*)(red + 4 * 512 + 4) = a1;
      if (lc == 0 && lg < 2) *(float2*)(ml + 16 + h2) = make_float2(M, L);
      lds_arrive(C_MERGED);
    }
  } else {
    lds_wait(C_MERGED, 1);
    const float* red = (const float*)(lds + L_RED) + 4 * 512 + ridx;
    const f32x4 ma = *(const f32x4*)((const float*)(lds + L_ML) + 16);
    const f32x4 a0 = *(const f32x4*)red, a1 = *(const f32x4*)(red + 4);
    const float m0 = dpp<0xA0>(m), m1 = dpp<0xF5>(m), l0 = dpp<0xA0>(l), l1 = dpp<0xF5>(l);
    const float mh = (lg & 1) ? m1 : m0, lh = (lg & 1) ? l1 : l0, Ma = (lg & 1) ? ma[2] : ma[0], La = (lg & 1) ? ma[3] : ma[1];
    const float M = fmaxf(mh, Ma), e0 = ex2(mh - M), e1 = ex2(Ma - M);
    const u32 rec = WS_RA + (g * 32 + s) * RA_BLK + ((lg & 1) * 16 + (lg >> 1) * 8 + (lc >> 1)) * 80;
    const u32x2 ep2 = *(const u32x2*)(lds + L_EP);
    u32x4 v0, v1;
#pragma unroll
    for (int e = 0; e < 4; ++e) { v0[e] = __float_as_uint(fmaf(o[e], e0, a0[e] * e1)); v1[e] = __float_as_uint(fmaf(o[4 + e], e0, a1[e] * e1)); }
    const u32 Mb = __float_as_uint(M), Lb = __float_as_uint(fmaf(lh, e0, La * e1));
    STAMP(3);
    if (VAR & 8192) wait_until(DLY);
    if (!(VAR & 32)) {
    __builtin_amdgcn_raw_buffer_store_b128(v0, rW, rec + (lc & 1) * 32, 0, AUX_SC1);
    __builtin_amdgcn_raw_buffer_store_b128(v1, rW, rec + (lc & 1) * 32 + 16, 0, AUX_SC1);
    u32 ca = 0, cb = 0;
#pragma unroll
    for (int e = 0; e < 4; ++e) { chk(ca, cb, v0[e], (lc & 1) * 8 + e); chk(ca, cb, v1[e], (lc & 1) * 8 + 4 + e); }
    ca += dppu<XOR1>(ca); cb ^= dppu<XOR1>(cb);
    ca += ep2[0]; cb ^= ep2[1];
    chk(ca, cb, Mb, 16); chk(ca, cb, Lb, 17);
    __builtin_amdgcn_raw_buffer_store_b128(u32x4{Mb, Lb, ca, cb}, rW, (lc & 1) ? OOB : rec + 64, 0, AUX_SC1);
    }
    MEMFENCE();
  }

  auto publish_c = [&](float y0, float y1) {
    const u32 b0 = __float_as_uint(y0), b1 = __float_as_uint(y1);
    u32 ca = 0, cb = 0;
    chk(ca, cb, b0, (lane & 7) * 2); chk(ca, cb, b1, (lane & 7) * 2 + 1);
    ca += dppu<XOR1>(ca); cb ^= dppu<XOR1>(cb); ca += dppu<XOR2>(ca); cb ^= dppu<XOR2>(cb); ca += dppu<XOR7>(ca); cb ^= dppu<XOR7>(cb);
    ca += (u32)epn; cb ^= (u32)(epn >> 32);
    const u32 z0 = dppu<XOR1>(b0), z1 = dppu<XOR1>(b1);
    const u32 rec = WS_RC + (w * 7 + (lane >> 3)) * RC_BLK + cslot * 128;
    const bool odd = lane & 1;
    const u32x4 ov = {odd ? ca : b0, odd ? cb : b1, odd ? ca : z0, odd ? cb : z1};
    __builtin_amdgcn_raw_buffer_store_b128(ov, rW, lane >= 56 ? OOB : !odd ? rec + (lane & 7) * 8 : (lane & 7) == 1 ? rec + 64 : OOB, 0, AUX_SC1);
    MEMFENCE();
  };
  auto gather_c = [&](int slot0, int nslot, u32 e0, u32 e1) {
    const u32 off = WS_RC + fid * RC_BLK + (slot0 + (lane >> 2)) * 128;
    u32x4 yv = {0u, 0u, 0u, 0u}; u32x2 cv;
    bool ok = (lane >> 2) >= nslot;
    for (u32 spins = 0; spins < SPIN_CAP; ++spins) {
      MEMFENCE();
      if (!ok) {
        yv = __builtin_amdgcn_raw_buffer_load_b128(rW, off + (lane & 3) * 16, 0, AUX_SC1);
        cv = __builtin_amdgcn_raw_buffer_load_b64(rW, off + 64, 0, AUX_SC1);
        u32 ca = 0, cb = 0;
#pragma unroll
        for (int e = 0; e < 4; ++e) chk(ca, cb, yv[e], (lane & 3) * 4 + e);
        ca += dppu<XOR1>(ca); cb ^= dppu<XOR1>(cb); ca += dppu<XOR2>(ca); cb ^= dppu<XOR2>(cb);
        u32 bad = ((ca + e0) ^ cv[0]) | ((cb ^ e1) ^ cv[1]);
        bad |= dppu<XOR1>(bad); bad |= dppu<XOR2>(bad);
        ok = bad == 0;
      }
      const int miss = __popcll(__ballot(!ok));
      if (miss == 0) break;
      for (int i = 8; i < miss; i += 8) __builtin_amdgcn_s_sleep(CSLEEP);
    }
    const float v0 = t32_sum(__uint_as_float(yv[0]), __uint_as_float(yv[2])), v1 = t32_sum(__uint_as_float(yv[1]), __uint_as_float(yv[3]));
    float t = t16_sum(v0, v1);
    t += dpp<ROR4>(t); t += dpp<XOR8>(t);
    if (!(lane & 12)) ((float*)(lds + L_FIN))[(w ^ 4) * 16 + (lane & 3) * 4 + lg] = t;
  };

  u32 wp[64];
  if (w < 4 && (has_mlp || cons)) {
    const u32 rowoff = lane < 56 ? lane * 16 : OOB;
    const int l2 = w * 56 + (lane < 56 ? lane : 0);
    if (CHT) wait_until(1200 + (((s - 16) & ~1) * 4 + (cons ? (s & 1) * 4 : 0) + w) * CHT);
    if (has_mlp) {
      const auto rW1 = __builtin_amdgcn_make_buffer_rsrc((void*)p.w1, 0, (ABL & 1) ? 0 : D * FF * 2, 0x00020000);
      const auto rW2 = __builtin_amdgcn_make_buffer_rsrc((void*)p.w2, 0, (ABL & 2) ? 0 : D * FF * 2, 0x00020000);
      const auto rB1 = __builtin_amdgcn_make_buffer_rsrc((void*)p.b1, 0, FF * 2, 0x00020000);
      const int u0 = mi * 32;
      u32x4 w1r[7], w2r[8];
#pragma unroll
      for (int i = 0; i < 7; ++i) w1r[i] = __builtin_amdgcn_raw_buffer_load_b128(rW1, (w * 16 + (lane >> 2)) * (FF * 2) + u0 * 2 + (lane & 3) * 16, i * 64 * (FF * 2), AUX_NT);
#pragma unroll
      for (int i = 0; i < 8; ++i) w2r[i] = __builtin_amdgcn_raw_buffer_load_b128(rW2, rowoff, (u0 + w * 8 + i) * 896, AUX_NT);
      const u32 b1r = __builtin_amdgcn_raw_buffer_load_b16(rB1, (u0 + w * 8 + (lane & 7)) * 2, 0, 0);
      gemv32(w1r);
      lds_arrive(C_HP);
      lds_wait(C_HP, 4);
      const float* hp = (const float*)(lds + L_QP) + w * 8 + (lane & 7);
      float hv = bfr(bfr((hp[0] + hp[32]) + (hp[64] + hp[96])) + bf2f_lo(b1r));
      hv = bfr(0.5f * hv * (1.f + tanhf(0.7978845608028654f * (hv + 0.044715f * hv * hv * hv))));
      float ym[8];
#pragma unroll
      for (int e = 0; e < 8; ++e) ym[e] = 0.f;
#pragma unroll
      for (int r = 0; r < 8; ++r) {
        const float hr = __builtin_bit_cast(float, __builtin_amdgcn_readlane(__builtin_bit_cast(int, hv), r));
#pragma unroll
        for (int e = 0; e < 4; ++e) { ym[2 * e] = fmaf(hr, bf2f_lo(w2r[r][e]), ym[2 * e]); ym[2 * e + 1] = fmaf(hr, bf2f_hi(w2r[r][e]), ym[2 * e + 1]); }
      }
      if (lane < 56) { float* y = (float*)(lds + L_YM) + w * 448 + lane * 8;
        *(f32x4*)y = f32x4{ym[0], ym[1], ym[2], ym[3]}; *(f32x4*)(y + 4) = f32x4{ym[4], ym[5], ym[6], ym[7]}; }
      lds_arrive(C_CHORE);
      lds_wait(C_CHORE, 4);
      const float2* y = (const float2*)(lds + L_YM) + l2;
      const float2 a = y[0], b = y[224], c = y[448], d = y[672];
      if (!(VAR & 64)) publish_c((a.x + b.x) + (c.x + d.x), (a.y + b.y) + (c.y + d.y));
    } else {
      const auto rWo = __builtin_amdgcn_make_buffer_rsrc((void*)p.wo, 0, (ABL & 4) ? 0 : D * HQ * DH * 2, 0x00020000);
      u32x4 wor[16];
#pragma unroll
      for (int i = 0; i < 16; ++i) wor[i] = __builtin_amdgcn_raw_buffer_load_b128(rWo, rowoff, (g * 512 + (s - 24) * 64 + w * 16 + i) * 896, AUX_NT);
      if (lane < 56) {
#pragma unroll
        for (int i = 0; i < 16; ++i) *(u32x4*)(lds + L_WO + (w * 16 + i) * 896 + lane * 16) = wor[i];
      }
      lds_arrive(C_CHORE);
      if (fin) {
        if (tid < 16) { const int col = fid * 16 + tid;
          ((float*)(lds + L_XB))[tid] = __bfloat162float(((const __hip_bfloat16*)p.x)[col]) + __bfloat162float(((const __hip_bfloat16*)p.b2)[col]); }
        wait_until(1200 + 64 * CHT + 400);
        if (!(VAR & (32 | 2048))) gather_c(64 + w * 16, w < 3 ? 16 : 8, (u32)epn, (u32)(epn >> 32));
      }
      lds_wait(C_CHORE, 4);
#pragma unroll
      for (int k = 0; k < 32; ++k) {
        const u32 a = ((const u32*)(lds + L_WO))[(2 * k) * 224 + l2], b = ((const u32*)(lds + L_WO))[(2 * k + 1) * 224 + l2];
        wp[2 * k] = __builtin_amdgcn_perm(b, a, 0x05040100u); wp[2 * k + 1] = __builtin_amdgcn_perm(b, a, 0x07060302u);
      }
      STAMPB(6, wp[63]);
    }
  }
  if (cons && !(VAR & (32 | 2048))) {
    bar();
    const u32x2 ep2 = *(const u32x2*)(lds + L_EP);
    if (w >= 4) {
      const u32 roff = WS_RA + (g * 32 + (lane >> 2)) * RA_BLK + ((s - 24) * 4 + w - 4) * 80;
      u32x4 ov[2], mv[2];
      bool ok[2] = {false, false};
      for (u32 spins = 0; spins < SPIN_CAP; ++spins) {
        MEMFENCE();
#pragma unroll
        for (int i = 0; i < 2; ++i) if (!ok[i]) {
          ov[i] = __builtin_amdgcn_raw_buffer_load_b128(rW, roff + i * 16 * RA_BLK + (lane & 3) * 16, 0, AUX_SC1);
          mv[i] = __builtin_amdgcn_raw_buffer_load_b128(rW, roff + i * 16 * RA_BLK + 64, 0, AUX_SC1);
        }
        int miss = 0;
#pragma unroll
        for (int i = 0; i < 2; ++i) {
          u32 ca = 0, cb = 0;
#pragma unroll
          for (int e = 0; e < 4; ++e) chk(ca, cb, ov[i][e], (lane & 3) * 4 + e);
          ca += dppu<XOR1>(ca); cb ^= dppu<XOR1>(cb); ca += dppu<XOR2>(ca); cb ^= dppu<XOR2>(cb);
          ca += ep2[0]; cb ^= ep2[1];
          chk(ca, cb, mv[i][0], 16); chk(ca, cb, mv[i][1], 17);
          u32 bad = (ca ^ mv[i][2]) | (cb ^ mv[i][3]);
          bad |= dppu<XOR1>(bad); bad |= dppu<XOR2>(bad);
          ok[i] = bad == 0;
          miss += __popcll(__ballot(!ok[i]));
        }
        if (miss == 0) break;
        for (int i = PTHR; i < miss; i += 4) __builtin_amdgcn_s_sleep(PSLEEP);
      }
      const float m0 = __uint_as_float(mv[0][0]), m1 = __uint_as_float(mv[1][0]);
      float M = fmaxf(m0, m1);
      M = fmaxf(M, dpp<ROR4>(M)); M = fmaxf(M, dpp<XOR8>(M)); M = x32_max(x16_max(M));
      const float w0 = ex2(m0 - M), w1 = ex2(m1 - M);
      float L = fmaf(w0, __uint_as_float(mv[0][1]), w1 * __uint_as_float(mv[1][1]));
      L += dpp<ROR4>(L); L += dpp<XOR8>(L); L = x32_sum(x16_sum(L));
      float oo[4];
#pragma unroll
      for (int e = 0; e < 4; ++e) oo[e] = fmaf(w0, __uint_as_float(ov[0][e]), w1 * __uint_as_float(ov[1][e]));
      oo[0] = t32_sum(oo[0], oo[2]); oo[1] = t32_sum(oo[1], oo[3]);
      float t = t16_sum(oo[0], oo[1]);
      t += dpp<ROR4>(t); t += dpp<XOR8>(t);
      if (!(lane & 12)) ((unsigned short*)(lds + L_O))[(w - 4) * 16 + (lane & 3) * 4 + lg] = (unsigned short)pk_bf16(t * __builtin_amdgcn_rcpf(L), 0.f);
    }
    bar();
    STAMP(5);
    if (w < 4) {
      float y0 = 0.f, y1 = 0.f, y2 = 0.f, y3 = 0.f;
#pragma unroll
      for (int k4 = 0; k4 < 8; ++k4) { const u32x4 op = ((const u32x4*)(lds + L_O))[k4];
#pragma unroll
        for (int e = 0; e < 4; e += 2) {
          y0 = dot2(wp[8 * k4 + 2 * e], op[e], y0); y1 = dot2(wp[8 * k4 + 2 * e + 1], op[e], y1);
          y2 = dot2(wp[8 * k4 + 2 * e + 2], op[e + 1], y2); y3 = dot2(wp[8 * k4 + 2 * e + 3], op[e + 1], y3);
        } }
      publish_c(y0 + y2, y1 + y3);
    }
    STAMP(6);
    if (fin) {
      if (w >= 4) gather_c((w - 4) * 16, 16, ep2[0], ep2[1]);
      bar();
      if (tid < 16) {
        const float* f = (const float*)(lds + L_FIN) + tid;
        const float r = (((const float*)(lds + L_XB))[tid] + ((f[64] + f[80]) + (f[96] + f[112]))) + ((f[0] + f[16]) + (f[32] + f[48]));
        ((unsigned short*)p.out)[fid * 16 + tid] = (unsigned short)pk_bf16(r, 0.f);
        if (fid == 0 && tid == 0) __builtin_amdgcn_raw_buffer_store_b64(ep2, rW, WS_EPOCH, 0, AUX_SC1);
      }
    }
  }
  if ((VAR & (32 | 2048)) && cons && w < 4 && wp[5] == 0x12345u) ((float*)p.ws)[77] = 1.f;
  if (bid == 0 && w == 7) { while ((u32)(__builtin_amdgcn_s_memrealtime() - tstart0) < 20000 + diag_c * 500) __builtin_amdgcn_s_sleep(4); }
}

extern "C" void kernel_launch(void* const* d_in, const int* in_sizes, int n_in, void* d_out, int out_size, void* d_ws, size_t ws_size, hipStream_t stream) {
  Params p;
  p.x = d_in[0]; p.k = d_in[1]; p.v = d_in[2]; p.wq = d_in[3]; p.wo = d_in[4];
  p.w1 = d_in[6]; p.b1 = d_in[7]; p.w2 = d_in[8]; p.b2 = d_in[9];
  p.out = d_out; p.ws = d_ws;
  hipLaunchKernelGGL(fused_decode, dim3(NWG), dim3(512), 0, stream, p);
}
```

```cpp
#include <hip/hip_runtime.h>
#include <hip/hip_bf16.h>
#include <stdint.h>

typedef unsigned u32;
typedef unsigned long long u64;
typedef u32 u32x4 __attribute__((ext_vector_type(4)));
typedef u32 u32x2 __attribute__((ext_vector_type(2)));
typedef float f32x4 __attribute__((ext_vector_type(4)));
typedef __bf16 bf16x8 __attribute__((ext_vector_type(8)));
typedef __bf16 bf16x2 __attribute__((ext_vector_type(2)));
typedef short s16x4 __attribute__((ext_vector_type(4)));
#define DI __device__ __forceinline__

#define STAMPS 0
#define TMASK 0x13a
#define QT 400
#define KVT 0
#define CHT 20
#define PSLEEP 2
#define ABL 0
#define VAR 0
#define PRET 2
#define DLY 7900
#define CSLEEP 6
#define PTHR 8
#define HEADMAP 1
#define HPRIO 3

constexpr int D = 448, HQ = 16, SK = 65536, DH = 256, FF = 1792;
constexpr int NWG = 256, NWAVE_TOTAL = 8;
constexpr int NPAIR = 64;
constexpr int NFIN = 28;
constexpr u32 WS_EPOCH = 0;
constexpr u32 WS_QG = 4096;
constexpr u32 WS_RA = WS_QG + 8 * 512 * 8;
constexpr u32 RA_BLK = 32 * 80;
constexpr u32 WS_RC = WS_RA + NWG * RA_BLK;
constexpr u32 RC_BLK = 128 * 128;
constexpr u32 WS_END = WS_RC + NFIN * RC_BLK;
constexpr u32 WS_STAMP = 2u << 20;
static_assert(WS_END <= WS_STAMP, "");
constexpr int AUX_NT = 2, AUX_SC1 = 16;
constexpr u32 SPIN_CAP = 1u << 15;
constexpr u32 OOB = 0x7fffff00u;

#define MEMFENCE() asm volatile("" ::: "memory")
#define SCHED() __builtin_amdgcn_sched_barrier(0)

DI float bf2f_lo(u32 x) { return __builtin_bit_cast(float, x << 16); }
DI float bf2f_hi(u32 x) { return __builtin_bit_cast(float, x & 0xffff0000u); }
typedef float f32x2 __attribute__((ext_vector_type(2)));
DI u32 pk_bf16(float a, float b) { const f32x2 f = {a, b}; return __builtin_bit_cast(u32, __builtin_convertvector(f, bf16x2)); }
DI float bfr(float a) { return bf2f_lo(pk_bf16(a, 0.f)); }
template <int CTRL> DI float dpp(float x) {
  return __builtin_bit_cast(float, __builtin_amdgcn_mov_dpp(__builtin_bit_cast(int, x), CTRL, 0xf, 0xf, true));
}
constexpr int XOR1 = 0xB1, XOR2 = 0x4E, XOR3 = 0x1B, XOR7 = 0x141, XOR8 = 0x128, ROR4 = 0x124;
DI float t32_sum(float u, float v) { auto s = __builtin_amdgcn_permlane32_swap(__float_as_uint(u), __float_as_uint(v), false, false); return __uint_as_float(s[0]) + __uint_as_float(s[1]); }
DI float t16_sum(float u, float v) { auto s = __builtin_amdgcn_permlane16_swap(__float_as_uint(u), __float_as_uint(v), false, false); return __uint_as_float(s[0]) + __uint_as_float(s[1]); }
DI float x16_sum(float x) { return t16_sum(x, x); }
DI float x32_sum(float x) { return t32_sum(x, x); }
DI float x16_max(float x) { auto s = __builtin_amdgcn_permlane16_swap(__float_as_uint(x), __float_as_uint(x), false, false); return fmaxf(__uint_as_float(s[0]), __uint_as_float(s[1])); }
DI float x32_max(float x) { auto s = __builtin_amdgcn_permlane32_swap(__float_as_uint(x), __float_as_uint(x), false, false); return fmaxf(__uint_as_float(s[0]), __uint_as_float(s[1])); }
DI float row_sum(float x) { x += dpp<XOR1>(x); x += dpp<XOR2>(x); x += dpp<XOR7>(x); x += dpp<XOR8>(x); return x; }
DI float wave_sum(float x) { return x32_sum(x16_sum(row_sum(x))); }
template <int CTRL> DI u32 dppu(u32 x) { return (u32)__builtin_amdgcn_mov_dpp((int)x, CTRL, 0xf, 0xf, true); }
DI u32 rotl(u32 x, u32 r) { return __builtin_amdgcn_alignbit(x, x, 32u - r); }
DI void chk(u32& ca, u32& cb, u32 v, u32 i) { ca += v; cb ^= rotl(v, i + 1); }
DI void bar() { asm volatile("s_waitcnt lgkmcnt(0)\n\ts_barrier" ::: "memory"); }
DI float dot2(u32 a, u32 b, float c) { return __builtin_amdgcn_fdot2_f32_bf16(__builtin_bit_cast(bf16x2, a), __builtin_bit_cast(bf16x2, b), c, false); }
DI float ex2(float x) { return __builtin_amdgcn_exp2f(x); }
template <u32 M> DI u32 pdep(u32 x) {
  u32 r = 0, k = 0;
#pragma unroll
  for (int b = 0; b < 12; ++b) if ((M >> b) & 1) { r |= ((x >> k) & 1u) << b; ++k; }
  return r;
}

struct Params {
  const void* x; const void* k; const void* v; const void* wq; const void* wo;
  const void* w1; const void* b1; const void* w2; const void* b2;
  void* out; void* ws;
};

constexpr int L_XN = 0;
constexpr int QSTR = 576;
constexpr int L_Q = L_XN + 2048;
constexpr int L_WO = L_Q + 2 * QSTR;
constexpr int L_QP = L_WO + 64 * 896;
constexpr int L_YM = L_QP + 512;
constexpr int L_RED = L_YM + 4 * 448 * 4;
constexpr int L_ML = L_RED + 8 * 2048;
constexpr int L_O = L_ML + 128;
constexpr int L_FIN = L_O + 128;
constexpr int L_XB = L_FIN + 512;
constexpr int L_EP = L_XB + 64;
constexpr int L_CNT = L_EP + 16;
constexpr int L_END = L_CNT + 64;
enum { C_QPART, C_QRDY, C_HP, C_CHORE, C_TURN, C_PART, C_MERGED };

__global__ __launch_bounds__(512, 2) void fused_decode(Params p) {
  __shared__ __attribute__((aligned(16))) char lds[L_END];
  const int tid = threadIdx.x, lane = tid & 63;
  const int w = __builtin_amdgcn_readfirstlane(tid >> 6);
  const int bid = blockIdx.x;
  const int g = HEADMAP == 0 ? bid & 7 : HEADMAP == 1 ? ((bid >> 6) << 1) | ((bid >> 2) & 1) : bid >> 5;
  const int s = HEADMAP == 0 ? bid >> 3 : HEADMAP == 1 ? (((bid >> 4) & 3) << 3) | ((bid & 3) << 1) | ((bid >> 3) & 1) : bid & 31;
  const int lg = lane >> 4, lc = lane & 15;
#define STAMPC(i)
#define STAMP(i)
#define STAMPB(i, v)
  STAMP(0);
  const auto rW = __builtin_amdgcn_make_buffer_rsrc(p.ws, 0, WS_END, 0x00020000);
  u64 epn = 0;
  u32* const cnt = (u32*)(lds + L_CNT);
  float* const xn = (float*)(lds + L_XN);
  auto lds_arrive = [&](int i) -> u32 {
    asm volatile("s_waitcnt lgkmcnt(0)" ::: "memory");
    u32 r = 0;
    if (lane == 0) r = __hip_atomic_fetch_add(cnt + i, 1u, __ATOMIC_RELAXED, __HIP_MEMORY_SCOPE_WORKGROUP);
    r = __builtin_amdgcn_readfirstlane(r);
    MEMFENCE();
    return r;
  };
  auto lds_wait = [&](int i, u32 v) { while (__hip_atomic_load(cnt + i, __ATOMIC_RELAXED, __HIP_MEMORY_SCOPE_WORKGROUP) != v) __builtin_amdgcn_s_sleep(1); MEMFENCE(); };
  const u64 tstart0 = __builtin_amdgcn_s_memrealtime();
  u64 tstart = tstart0;
  auto wait_until = [&](u32 ticks) { while ((u32)(__builtin_amdgcn_s_memrealtime() - tstart) < ticks) __builtin_amdgcn_s_sleep(1); };
  const bool q_prod = s < 16, has_mlp = s >= 16 && s < (g < 4 ? 24 : 22), cons = s >= 24;
  const int mi = (g < 4 ? g * 8 : 8 + g * 6) + s - 16;
  const int cslot = cons ? (s - 24) * 8 + g : 64 + mi;
  const int fid = (s - 24) * 8 + g;
  const bool fin = cons && fid < NFIN;
  auto gemv32 = [&](const u32x4 (&wr)[7]) {
    float a[8];
#pragma unroll
    for (int e = 0; e < 8; ++e) a[e] = 0.f;
#pragma unroll
    for (int i = 0; i < 7; ++i) { const float xx = xn[16 * (w + 4 * i) + (lane >> 2)];
#pragma unroll
      for (int e = 0; e < 4; ++e) { a[2 * e] = fmaf(xx, bf2f_lo(wr[i][e]), a[2 * e]); a[2 * e + 1] = fmaf(xx, bf2f_hi(wr[i][e]), a[2 * e + 1]); } }
#pragma unroll
    for (int e = 0; e < 4; ++e) a[e] = t32_sum(a[e], a[e + 4]);
#pragma unroll
    for (int e = 0; e < 2; ++e) a[e] = t16_sum(a[e], a[e + 2]);
    const float t1 = a[0] + dpp<XOR8>(a[0]), t2 = a[1] + dpp<XOR8>(a[1]);
    float t = (lane & 8) ? t2 : t1;
    t += dpp<XOR7>(dpp<XOR3>(t));
    if (!(lane & 4)) ((float*)(lds + L_QP))[w * 32 + (lane & 3) * 8 + (lane >> 3)] = t;
  };
  if (PRET && w < 4) {
    const u32x4 z = {0u, 0u, 0u, 0u};
    __builtin_amdgcn_raw_buffer_store_b128(z, rW, tid < 160 ? WS_RA + (g * 32 + s) * RA_BLK + tid * 16 : OOB, 0, AUX_SC1);
    if (PRET > 1 && (cons || has_mlp)) __builtin_amdgcn_raw_buffer_store_b128(z, rW, lane < 56 ? WS_RC + (w * 7 + (lane >> 3)) * RC_BLK + cslot * 128 + (lane & 7) * 16 : OOB, 0, AUX_SC1);
    MEMFENCE();
  }
  u32x4 xr, wq[7];
  u32x2 ep = {0u, 0u};
  if (w < 4 || w == 5) ep = __builtin_amdgcn_raw_buffer_load_b64(rW, WS_EPOCH, 0, AUX_SC1);
  u32 tag_lo = 0, tag_hi = 0;
  auto take_epoch = [&]() {
    asm volatile("" : "+v"(ep));
    const u64 ep64 = ((u64)(u32)__builtin_amdgcn_readfirstlane(ep[1]) << 32) | (u32)__builtin_amdgcn_readfirstlane(ep[0]);
    epn = ep64 * 6364136223846793005ull + 1442695040888963407ull;
    tag_lo = (u32)epn << 16; tag_hi = (u32)(epn >> 16);
  };
  if (w < 4) {
    __builtin_amdgcn_s_setprio(HPRIO);
    const auto rX = __builtin_amdgcn_make_buffer_rsrc((void*)p.x, 0, D * 2, 0x00020000);
    const auto rWq = __builtin_amdgcn_make_buffer_rsrc((void*)p.wq, 0, ((ABL & 8) || !q_prod) ? 0 : D * HQ * DH * 2, 0x00020000);
    xr = __builtin_amdgcn_raw_buffer_load_b128(rX, lane * 16, 0, 0);
    const u32 wqo = (w * 16 + (lane >> 2)) * 8192 + (g * 512 + s * 32) * 2 + (lane & 3) * 16;
#pragma unroll
    for (int i = 0; i < 7; ++i) wq[i] = __builtin_amdgcn_raw_buffer_load_b128(rWq, wqo, i * 64 * 8192, AUX_NT);
  }
  if (w != 4) {
    if (tid < 16) cnt[tid] = 0;
    bar();
  }
  if (__builtin_expect(w < 4, 0)) {
    {
      float xv[8];
#pragma unroll
      for (int e = 0; e < 4; ++e) { xv[2 * e] = bf2f_lo(xr[e]); xv[2 * e + 1] = bf2f_hi(xr[e]); }
      STAMPB(1, xv[0]);
      const float mean = wave_sum(((xv[0] + xv[1]) + (xv[2] + xv[3])) + ((xv[4] + xv[5]) + (xv[6] + xv[7]))) * (1.f / D);
      float sq = 0.f;
#pragma unroll
      for (int e = 0; e < 8; ++e) { const float d = xv[e] - mean; sq = fmaf(d, d, sq); }
      sq = lane < 56 ? sq : 0.f;
      const float var = wave_sum(sq) * (1.f / D);
      const float mb = bfr(mean), den = bfr(__builtin_amdgcn_sqrtf(bfr(bfr(var) + 1e-5f)));
      const float rden = lane < 56 ? __builtin_amdgcn_rcpf(den) : 0.f;
      f32x4 o[2];
#pragma unroll
      for (int e = 0; e < 8; ++e) o[e >> 2][e & 3] = bfr(bfr(xv[e] - mb) * rden);
      *(f32x4*)(xn + lane * 8) = o[0]; *(f32x4*)(xn + lane * 8 + 4) = o[1];
    }
    take_epoch();
    if (q_prod) {
      gemv32(wq);
      STAMPB(2, lane);
      if (lds_arrive(C_QPART) == 3) {
        const float* qp = (const float*)(lds + L_QP) + (lane & 31);
        const float q = (qp[0] + qp[32]) + (qp[64] + qp[96]);
        const u32x2 gr = {tag_lo | (pk_bf16(q, 0.f) & 0xffffu), tag_hi};
        __builtin_amdgcn_raw_buffer_store_b64(gr, rW, lane < 32 ? WS_QG + (g * 512 + s * 32 + lane) * 8 : OOB, 0, AUX_SC1);
      }
    }
    MEMFENCE();
    STAMPB(3, lane);
    __builtin_amdgcn_s_setprio(0);
  }

  float m = -INFINITY, l = 0.f, o[8];
  {
    const auto rK = __builtin_amdgcn_make_buffer_rsrc((void*)p.k, 0, 0x10000000, 0x00020000);
    const auto rV = __builtin_amdgcn_make_buffer_rsrc((void*)p.v, 0, 0x10000000, 0x00020000);
    u32x4 kb[2][8], vb[2][8];
    const u32 koff = (4 * (lc & 3) + (lc >> 2)) * 512 + lg * 16;
    const u32 voff = lg * 512 + lc * 16;
    const u32 tbase = g * (u32)(SK * 512) + (pdep<TMASK>(s) << 13);
    auto toff = [&](u32 i) -> u32 { return tbase + (pdep<~TMASK & 0xfff>(i) << 13); };
    auto loadK = [&](int d, u32 so) {
      u32 oo = koff; asm volatile("" : "+v"(oo));
#pragma unroll
      for (int j = 0; j < 8; ++j) kb[d][j] = __builtin_amdgcn_raw_buffer_load_b128(rK, oo + j * 64, so, AUX_NT);
      MEMFENCE(); SCHED();
    };
    auto loadV = [&](int d, u32 so) {
      u32 oo = voff; asm volatile("" : "+v"(oo));
#pragma unroll
      for (int j = 0; j < 8; ++j) vb[d][j] = __builtin_amdgcn_raw_buffer_load_b128(rV, oo + ((j >> 1) & 1) * 2048 + (j & 1) * 256, so + (j >> 2) * 4096, AUX_NT);
      MEMFENCE(); SCHED();
    };
    const u32 pair0 = w ^ 4;
    {
      const u32 so0 = toff(2 * pair0), so1 = toff(2 * pair0 + 1);
      asm volatile("" :: "s"(so0), "s"(so1), "v"(koff), "v"(voff));
      if (pair0) lds_wait(C_TURN, pair0); else if (KVT && q_prod) wait_until(KVT);
      STAMPC(1);
      if (w >= 4) STAMPB(2, lane);
      loadK(0, so0);
      if (w == 4) bar();
      loadV(0, so0); loadK(1, so1); loadV(1, so1);
      if (lane == 0) __hip_atomic_store(cnt + C_TURN, pair0 + 1, __ATOMIC_RELAXED, __HIP_MEMORY_SCOPE_WORKGROUP);
      MEMFENCE();
    }
    STAMPC(2);
    if (w >= 4) STAMPB(1, lane);
    if (w == 5) {
      take_epoch();
      if (!(VAR & 8)) {
        const u32 qgoff = WS_QG + (g * 512 + lane * 2) * 8;
        u32x4 q[4];
        bool ok[4] = {false, false, false, false};
        wait_until(QT);
        for (u32 spins = 0; spins < SPIN_CAP; ++spins) {
          MEMFENCE();
#pragma unroll
          for (int i = 0; i < 4; ++i) if (!ok[i]) q[i] = __builtin_amdgcn_raw_buffer_load_b128(rW, qgoff + i * 1024, 0, AUX_SC1);
#pragma unroll
          for (int i = 0; i < 4; ++i) ok[i] = ((q[i][0] & 0xffff0000u) == tag_lo) & (q[i][1] == tag_hi) & ((q[i][2] & 0xffff0000u) == tag_lo) & (q[i][3] == tag_hi);
          if (__all(ok[0] & ok[1] & ok[2] & ok[3])) break;
        }
#pragma unroll
        for (int i = 0; i < 4; ++i) *(u32*)(lds + L_Q + (i >> 1) * QSTR + (i & 1) * 256 + lane * 4) = (q[i][0] & 0xffffu) | (q[i][2] << 16);
      }
      if (lane == 0) *(u64*)(lds + L_EP) = epn;
      lds_arrive(C_QRDY);
      STAMPB(4, lane);
    }
    lds_wait(C_QRDY, 1);
    if (w == 4) lds_wait(C_TURN, 8);
    STAMP(1); STAMPB(5, lane);
    bf16x8 qf[8];
#pragma unroll
    for (int j = 0; j < 8; ++j) qf[j] = *(const bf16x8*)(lds + L_Q + (lc & 1) * QSTR + lg * 16 + j * 64);
    constexpr float C2 = 0.0625f * 1.4426950408889634f;
    f32x4 acc[16];
#pragma unroll
    for (int i = 0; i < 16; ++i) acc[i] = f32x4{0.f, 0.f, 0.f, 0.f};
    auto qk = [&](int d) -> f32x4 {
      f32x4 sc = {0.f, 0.f, 0.f, 0.f};
#pragma unroll
      for (int j = 0; j < 8; ++j) sc = __builtin_amdgcn_mfma_f32_16x16x32_bf16(__builtin_bit_cast(bf16x8, kb[d][j]), qf[j], sc, 0, 0, 0);
      asm volatile("" : "+v"(sc) :: "memory"); SCHED();
      return sc;
    };
    auto pv = [&](int d, f32x4 sc) {
      const u32 sa = pk_bf16(sc[0], sc[1]), sb = pk_bf16(sc[2], sc[3]);
      const float t0 = bf2f_lo(sa), t1 = bf2f_hi(sa), t2 = bf2f_lo(sb), t3 = bf2f_hi(sb);
      const float tl = fmaxf(fmaxf(t0, t1), fmaxf(t2, t3)) * C2;
      if (__any(tl > m)) {
        const float mn = fmaxf(m, x32_max(x16_max(tl))), al = ex2(m - mn);
        m = mn; l *= al;
        const float a0 = dpp<0xA0>(al), a1 = dpp<0xF5>(al);
#pragma unroll
        for (int i = 0; i < 16; ++i) { acc[i][0] *= a0; acc[i][1] *= a1; }
      }
      const float p0 = ex2(fmaf(t0, C2, -m)), p1 = ex2(fmaf(t1, C2, -m)), p2 = ex2(fmaf(t2, C2, -m)), p3 = ex2(fmaf(t3, C2, -m));
      l += (p0 + p1) + (p2 + p3);
      const u32x2 pp = {pk_bf16(p0, p1), pk_bf16(p2, p3)};
      const s16x4 pf = __builtin_bit_cast(s16x4, pp);
#pragma unroll
      for (int hf = 0; hf < 2; ++hf)
#pragma unroll
        for (int e = 0; e < 8; ++e) {
          const u32 sel = (e & 1) ? 0x07060302u : 0x05040100u;
          const u32x2 bb = {__builtin_amdgcn_perm(vb[d][2 + hf][e >> 1], vb[d][hf][e >> 1], sel), __builtin_amdgcn_perm(vb[d][6 + hf][e >> 1], vb[d][4 + hf][e >> 1], sel)};
          acc[hf * 8 + e] = __builtin_amdgcn_mfma_f32_16x16x16bf16_1k(pf, __builtin_bit_cast(s16x4, bb), acc[hf * 8 + e], 0, 0, 0);
        }
      MEMFENCE(); SCHED();
    };
    if (w == 4) {
      for (u32 tp = 8; tp < NPAIR; ++tp) {
#pragma unroll
        for (int d = 0; d < 2; ++d) {
          const u32 so = toff(2 * tp + d);
          const f32x4 sc = qk(d);
          loadK(d, so); pv(d, sc); loadV(d, so);
        }
      }
    }
#pragma unroll
    for (int d = 0; d < 2; ++d) { const f32x4 sc = qk(d); pv(d, sc); }
    STAMP(2);
    l = x32_sum(x16_sum(l));
#pragma unroll
    for (int e = 0; e < 8; ++e) { const float lo = (lg & 1) ? acc[e][1] : acc[e][0], hi = (lg & 1) ? acc[8 + e][1] : acc[8 + e][0]; o[e] = (lg >> 1) ? hi : lo; }
  }
  const int ridx = (lg & 1) * 256 + (lg >> 1) * 128 + lc * 8;
  if (w != 4) {
    float* red = (float*)(lds + L_RED) + ridx;
    float* ml = (float*)(lds + L_ML);
    *(f32x4*)(red + w * 512) = f32x4{o[0], o[1], o[2], o[3]}; *(f32x4*)(red + w * 512 + 4) = f32x4{o[4], o[5], o[6], o[7]};
    if (lane < 2) *(float2*)(ml + w * 4 + lane * 2) = make_float2(m, l);
    if (lds_arrive(C_PART) == 6) {
      const int h2 = (lg & 1) * 2;
      float M = -INFINITY, L = 0.f;
      f32x4 a0 = {0.f, 0.f, 0.f, 0.f}, a1 = a0;
#pragma unroll
      for (int i = 0; i < 8; ++i) if (i != 4) M = fmaxf(M, ml[i * 4 + h2]);
#pragma unroll
      for (int i = 0; i < 8; ++i) if (i != 4) { const float e = ex2(ml[i * 4 + h2] - M);
        L = fmaf(ml[i * 4 + h2 + 1], e, L); a0 += *(const f32x4*)(red + i * 512) * e; a1 += *(const f32x4*)(red + i * 512 + 4) * e; }
      *(f32x4*)(red + 4 * 512) = a0; *(f32x4*)(red + 4 * 512 + 4) = a1;
      if (lc == 0 && lg < 2) *(float2*)(ml + 16 + h2) = make_float2(M, L);
      lds_arrive(C_MERGED);
    }
  } else {
    lds_wait(C_MERGED, 1);
    const float* red = (const float*)(lds + L_RED) + 4 * 512 + ridx;
    const f32x4 ma = *(const f32x4*)((const float*)(lds + L_ML) + 16);
    const f32x4 a0 = *(const f32x4*)red, a1 = *(const f32x4*)(red + 4);
    const float m0 = dpp<0xA0>(m), m1 = dpp<0xF5>(m), l0 = dpp<0xA0>(l), l1 = dpp<0xF5>(l);
    const float mh = (lg & 1) ? m1 : m0, lh = (lg & 1) ? l1 : l0, Ma = (lg & 1) ? ma[2] : ma[0], La = (lg & 1) ? ma[3] : ma[1];
    const float M = fmaxf(mh, Ma), e0 = ex2(mh - M), e1 = ex2(Ma - M);
    const u32 rec = WS_RA + (g * 32 + s) * RA_BLK + ((lg & 1) * 16 + (lg >> 1) * 8 + (lc >> 1)) * 80;
    const u32x2 ep2 = *(const u32x2*)(lds + L_EP);
    u32x4 v0, v1;
#pragma unroll
    for (int e = 0; e < 4; ++e) { v0[e] = __float_as_uint(fmaf(o[e], e0, a0[e] * e1)); v1[e] = __float_as_uint(fmaf(o[4 + e], e0, a1[e] * e1)); }
    const u32 Mb = __float_as_uint(M), Lb = __float_as_uint(fmaf(lh, e0, La * e1));
    STAMP(3);
    if (VAR & 8192) wait_until(DLY);
    if (!(VAR & 32)) {
    __builtin_amdgcn_raw_buffer_store_b128(v0, rW, rec + (lc & 1) * 32, 0, AUX_SC1);
    __builtin_amdgcn_raw_buffer_store_b128(v1, rW, rec + (lc & 1) * 32 + 16, 0, AUX_SC1);
    u32 ca = 0, cb = 0;
#pragma unroll
    for (int e = 0; e < 4; ++e) { chk(ca, cb, v0[e], (lc & 1) * 8 + e); chk(ca, cb, v1[e], (lc & 1) * 8 + 4 + e); }
    ca += dppu<XOR1>(ca); cb ^= dppu<XOR1>(cb);
    ca += ep2[0]; cb ^= ep2[1];
    chk(ca, cb, Mb, 16); chk(ca, cb, Lb, 17);
    __builtin_amdgcn_raw_buffer_store_b128(u32x4{Mb, Lb, ca, cb}, rW, (lc & 1) ? OOB : rec + 64, 0, AUX_SC1);
    }
    MEMFENCE();
  }

  auto publish_c = [&](float y0, float y1) {
    const u32 b0 = __float_as_uint(y0), b1 = __float_as_uint(y1);
    u32 ca = 0, cb = 0;
    chk(ca, cb, b0, (lane & 7) * 2); chk(ca, cb, b1, (lane & 7) * 2 + 1);
    ca += dppu<XOR1>(ca); cb ^= dppu<XOR1>(cb); ca += dppu<XOR2>(ca); cb ^= dppu<XOR2>(cb); ca += dppu<XOR7>(ca); cb ^= dppu<XOR7>(cb);
    ca += (u32)epn; cb ^= (u32)(epn >> 32);
    const u32 z0 = dppu<XOR1>(b0), z1 = dppu<XOR1>(b1);
    const u32 rec = WS_RC + (w * 7 + (lane >> 3)) * RC_BLK + cslot * 128;
    const bool odd = lane & 1;
    const u32x4 ov = {odd ? ca : b0, odd ? cb : b1, odd ? ca : z0, odd ? cb : z1};
    __builtin_amdgcn_raw_buffer_store_b128(ov, rW, lane >= 56 ? OOB : !odd ? rec + (lane & 7) * 8 : (lane & 7) == 1 ? rec + 64 : OOB, 0, AUX_SC1);
    MEMFENCE();
  };
  auto gather_c = [&](int slot0, int nslot, u32 e0, u32 e1) {
    const u32 off = WS_RC + fid * RC_BLK + (slot0 + (lane >> 2)) * 128;
    u32x4 yv = {0u, 0u, 0u, 0u}; u32x2 cv;
    bool ok = (lane >> 2) >= nslot;
    for (u32 spins = 0; spins < SPIN_CAP; ++spins) {
      MEMFENCE();
      if (!ok) {
        yv = __builtin_amdgcn_raw_buffer_load_b128(rW, off + (lane & 3) * 16, 0, AUX_SC1);
        cv = __builtin_amdgcn_raw_buffer_load_b64(rW, off + 64, 0, AUX_SC1);
        u32 ca = 0, cb = 0;
#pragma unroll
        for (int e = 0; e < 4; ++e) chk(ca, cb, yv[e], (lane & 3) * 4 + e);
        ca += dppu<XOR1>(ca); cb ^= dppu<XOR1>(cb); ca += dppu<XOR2>(ca); cb ^= dppu<XOR2>(cb);
        u32 bad = ((ca + e0) ^ cv[0]) | ((cb ^ e1) ^ cv[1]);
        bad |= dppu<XOR1>(bad); bad |= dppu<XOR2>(bad);
        ok = bad == 0;
      }
      const int miss = __popcll(__ballot(!ok));
      if (miss == 0) break;
      for (int i = 8; i < miss; i += 8) __builtin_amdgcn_s_sleep(CSLEEP);
    }
    const float v0 = t32_sum(__uint_as_float(yv[0]), __uint_as_float(yv[2])), v1 = t32_sum(__uint_as_float(yv[1]), __uint_as_float(yv[3]));
    float t = t16_sum(v0, v1);
    t += dpp<ROR4>(t); t += dpp<XOR8>(t);
    if (!(lane & 12)) ((float*)(lds + L_FIN))[(w ^ 4) * 16 + (lane & 3) * 4 + lg] = t;
  };

  u32 wp[64];
  if (w < 4 && (has_mlp || cons)) {
    const u32 rowoff = lane < 56 ? lane * 16 : OOB;
    const int l2 = w * 56 + (lane < 56 ? lane : 0);
    if (CHT) wait_until(1200 + (((s - 16) & ~1) * 4 + (cons ? (s & 1) * 4 : 0) + w) * CHT);
    if (has_mlp) {
      const auto rW1 = __builtin_amdgcn_make_buffer_rsrc((void*)p.w1, 0, (ABL & 1) ? 0 : D * FF * 2, 0x00020000);
      const auto rW2 = __builtin_amdgcn_make_buffer_rsrc((void*)p.w2, 0, (ABL & 2) ? 0 : D * FF * 2, 0x00020000);
      const auto rB1 = __builtin_amdgcn_make_buffer_rsrc((void*)p.b1, 0, FF * 2, 0x00020000);
      const int u0 = mi * 32;
      u32x4 w1r[7], w2r[8];
#pragma unroll
      for (int i = 0; i < 7; ++i) w1r[i] = __builtin_amdgcn_raw_buffer_load_b128(rW1, (w * 16 + (lane >> 2)) * (FF * 2) + u0 * 2 + (lane & 3) * 16, i * 64 * (FF * 2), AUX_NT);
#pragma unroll
      for (int i = 0; i < 8; ++i) w2r[i] = __builtin_amdgcn_raw_buffer_load_b128(rW2, rowoff, (u0 + w * 8 + i) * 896, AUX_NT);
      const u32 b1r = __builtin_amdgcn_raw_buffer_load_b16(rB1, (u0 + w * 8 + (lane & 7)) * 2, 0, 0);
      gemv32(w1r);
      lds_arrive(C_HP);
      lds_wait(C_HP, 4);
      const float* hp = (const float*)(lds + L_QP) + w * 8 + (lane & 7);
      float hv = bfr(bfr((hp[0] + hp[32]) + (hp[64] + hp[96])) + bf2f_lo(b1r));
      hv = bfr(0.5f * hv * (1.f + tanhf(0.7978845608028654f * (hv + 0.044715f * hv * hv * hv))));
      float ym[8];
#pragma unroll
      for (int e = 0; e < 8; ++e) ym[e] = 0.f;
#pragma unroll
      for (int r = 0; r < 8; ++r) {
        const float hr = __builtin_bit_cast(float, __builtin_amdgcn_readlane(__builtin_bit_cast(int, hv), r));
#pragma unroll
        for (int e = 0; e < 4; ++e) { ym[2 * e] = fmaf(hr, bf2f_lo(w2r[r][e]), ym[2 * e]); ym[2 * e + 1] = fmaf(hr, bf2f_hi(w2r[r][e]), ym[2 * e + 1]); }
      }
      if (lane < 56) { float* y = (float*)(lds + L_YM) + w * 448 + lane * 8;
        *(f32x4*)y = f32x4{ym[0], ym[1], ym[2], ym[3]}; *(f32x4*)(y + 4) = f32x4{ym[4], ym[5], ym[6], ym[7]}; }
      lds_arrive(C_CHORE);
      lds_wait(C_CHORE, 4);
      const float2* y = (const float2*)(lds + L_YM) + l2;
      const float2 a = y[0], b = y[224], c = y[448], d = y[672];
      if (!(VAR & 64)) publish_c((a.x + b.x) + (c.x + d.x), (a.y + b.y) + (c.y + d.y));
    } else {
      const auto rWo = __builtin_amdgcn_make_buffer_rsrc((void*)p.wo, 0, (ABL & 4) ? 0 : D * HQ * DH * 2, 0x00020000);
      u32x4 wor[16];
#pragma unroll
      for (int i = 0; i < 16; ++i) wor[i] = __builtin_amdgcn_raw_buffer_load_b128(rWo, rowoff, (g * 512 + (s - 24) * 64 + w * 16 + i) * 896, AUX_NT);
      if (lane < 56) {
#pragma unroll
        for (int i = 0; i < 16; ++i) *(u32x4*)(lds + L_WO + (w * 16 + i) * 896 + lane * 16) = wor[i];
      }
      lds_arrive(C_CHORE);
      if (fin) {
        if (tid < 16) { const int col = fid * 16 + tid;
          ((float*)(lds + L_XB))[tid] = __bfloat162float(((const __hip_bfloat16*)p.x)[col]) + __bfloat162float(((const __hip_bfloat16*)p.b2)[col]); }
        wait_until(1200 + 64 * CHT + 400);
        if (!(VAR & (32 | 2048))) gather_c(64 + w * 16, w < 3 ? 16 : 8, (u32)epn, (u32)(epn >> 32));
      }
      lds_wait(C_CHORE, 4);
#pragma unroll
      for (int k = 0; k < 32; ++k) {
        const u32 a = ((const u32*)(lds + L_WO))[(2 * k) * 224 + l2], b = ((const u32*)(lds + L_WO))[(2 * k + 1) * 224 + l2];
        wp[2 * k] = __builtin_amdgcn_perm(b, a, 0x05040100u); wp[2 * k + 1] = __builtin_amdgcn_perm(b, a, 0x07060302u);
      }
      STAMPB(6, wp[63]);
    }
  }
  if (cons && !(VAR & (32 | 2048))) {
    bar();
    const u32x2 ep2 = *(const u32x2*)(lds + L_EP);
    if (w >= 4) {
      const u32 roff = WS_RA + (g * 32 + (lane >> 2)) * RA_BLK + ((s - 24) * 4 + w - 4) * 80;
      u32x4 ov[2], mv[2];
      bool ok[2] = {false, false};
      for (u32 spins = 0; spins < SPIN_CAP; ++spins) {
        MEMFENCE();
#pragma unroll
        for (int i = 0; i < 2; ++i) if (!ok[i]) {
          ov[i] = __builtin_amdgcn_raw_buffer_load_b128(rW, roff + i * 16 * RA_BLK + (lane & 3) * 16, 0, AUX_SC1);
          mv[i] = __builtin_amdgcn_raw_buffer_load_b128(rW, roff + i * 16 * RA_BLK + 64, 0, AUX_SC1);
        }
        int miss = 0;
#pragma unroll
        for (int i = 0; i < 2; ++i) {
          u32 ca = 0, cb = 0;
#pragma unroll
          for (int e = 0; e < 4; ++e) chk(ca, cb, ov[i][e], (lane & 3) * 4 + e);
          ca += dppu<XOR1>(ca); cb ^= dppu<XOR1>(cb); ca += dppu<XOR2>(ca); cb ^= dppu<XOR2>(cb);
          ca += ep2[0]; cb ^= ep2[1];
          chk(ca, cb, mv[i][0], 16); chk(ca, cb, mv[i][1], 17);
          u32 bad = (ca ^ mv[i][2]) | (cb ^ mv[i][3]);
          bad |= dppu<XOR1>(bad); bad |= dppu<XOR2>(bad);
          ok[i] = bad == 0;
          miss += __popcll(__ballot(!ok[i]));
        }
        if (miss == 0) break;
        for (int i = PTHR; i < miss; i += 4) __builtin_amdgcn_s_sleep(PSLEEP);
      }
      const float m0 = __uint_as_float(mv[0][0]), m1 = __uint_as_float(mv[1][0]);
      float M = fmaxf(m0, m1);
      M = fmaxf(M, dpp<ROR4>(M)); M = fmaxf(M, dpp<XOR8>(M)); M = x32_max(x16_max(M));
      const float w0 = ex2(m0 - M), w1 = ex2(m1 - M);
      float L = fmaf(w0, __uint_as_float(mv[0][1]), w1 * __uint_as_float(mv[1][1]));
      L += dpp<ROR4>(L); L += dpp<XOR8>(L); L = x32_sum(x16_sum(L));
      float oo[4];
#pragma unroll
      for (int e = 0; e < 4; ++e) oo[e] = fmaf(w0, __uint_as_float(ov[0][e]), w1 * __uint_as_float(ov[1][e]));
      oo[0] = t32_sum(oo[0], oo[2]); oo[1] = t32_sum(oo[1], oo[3]);
      float t = t16_sum(oo[0], oo[1]);
      t += dpp<ROR4>(t); t += dpp<XOR8>(t);
      if (!(lane & 12)) ((unsigned short*)(lds + L_O))[(w - 4) * 16 + (lane & 3) * 4 + lg] = (unsigned short)pk_bf16(t * __builtin_amdgcn_rcpf(L), 0.f);
    }
    bar();
    STAMP(5);
    if (w < 4) {
      float y0 = 0.f, y1 = 0.f, y2 = 0.f, y3 = 0.f;
#pragma unroll
      for (int k4 = 0; k4 < 8; ++k4) { const u32x4 op = ((const u32x4*)(lds + L_O))[k4];
#pragma unroll
        for (int e = 0; e < 4; e += 2) {
          y0 = dot2(wp[8 * k4 + 2 * e], op[e], y0); y1 = dot2(wp[8 * k4 + 2 * e + 1], op[e], y1);
          y2 = dot2(wp[8 * k4 + 2 * e + 2], op[e + 1], y2); y3 = dot2(wp[8 * k4 + 2 * e + 3], op[e + 1], y3);
        } }
      publish_c(y0 + y2, y1 + y3);
    }
    STAMP(6);
    if (fin) {
      if (w >= 4) gather_c((w - 4) * 16, 16, ep2[0], ep2[1]);
      bar();
      if (tid < 16) {
        const float* f = (const float*)(lds + L_FIN) + tid;
        const float r = (((const float*)(lds + L_XB))[tid] + ((f[64] + f[80]) + (f[96] + f[112]))) + ((f[0] + f[16]) + (f[32] + f[48]));
        __hip_atomic_store((unsigned short*)p.out + fid * 16 + tid, (unsigned short)pk_bf16(r, 0.f), __ATOMIC_RELAXED, __HIP_MEMORY_SCOPE_AGENT);
        if (fid == 0 && tid == 0) __builtin_amdgcn_raw_buffer_store_b64(ep2, rW, WS_EPOCH, 0, AUX_SC1);
      }
    }
  }
  if ((VAR & (32 | 2048)) && cons && w < 4 && wp[5] == 0x12345u) ((float*)p.ws)[77] = 1.f;
  if (bid == 0 && w == 7) { const u32 dur = (u32)(__builtin_amdgcn_s_memrealtime() - tstart0); (void)dur; while ((u32)(__builtin_amdgcn_s_memrealtime() - tstart0) < 12000) __builtin_amdgcn_s_sleep(4); }
}

extern "C" void kernel_launch(void* const* d_in, const int* in_sizes, int n_in, void* d_out, int out_size, void* d_ws, size_t ws_size, hipStream_t stream) {
  Params p;
  p.x = d_in[0]; p.k = d_in[1]; p.v = d_in[2]; p.wq = d_in[3]; p.wo = d_in[4];
  p.w1 = d_in[6]; p.b1 = d_in[7]; p.w2 = d_in[8]; p.b2 = d_in[9];
  p.out = d_out; p.ws = d_ws;
  hipLaunchKernelGGL(fused_decode, dim3(NWG), dim3(512), 0, stream, p);
}
```

```cpp
#include <hip/hip_runtime.h>
#include <hip/hip_bf16.h>
#include <stdint.h>

typedef unsigned u32;
typedef unsigned long long u64;
typedef u32 u32x4 __attribute__((ext_vector_type(4)));
typedef u32 u32x2 __attribute__((ext_vector_type(2)));
typedef float f32x4 __attribute__((ext_vector_type(4)));
typedef __bf16 bf16x8 __attribute__((ext_vector_type(8)));
typedef __bf16 bf16x2 __attribute__((ext_vector_type(2)));
typedef short s16x4 __attribute__((ext_vector_type(4)));
#define DI __device__ __forceinline__

#define STAMPS 0
#define TMASK 0x13a
#define QT 400
#define KVT 0
#define CHT 20
#define PSLEEP 2
#define ABL 0
#define VAR 0
#define PRET 2
#define DLY 7900
#define CSLEEP 6
#define PTHR 8
#define HEADMAP 1
#define KVAUX 3
#define HPRIO 3

constexpr int D = 448, HQ = 16, SK = 65536, DH = 256, FF = 1792;
constexpr int NWG = 256, NWAVE_TOTAL = 8;
constexpr int NPAIR = 64;
constexpr int NFIN = 28;
constexpr u32 WS_EPOCH = 0;
constexpr u32 WS_QG = 4096;
constexpr u32 WS_RA = WS_QG + 8 * 512 * 8;
constexpr u32 RA_BLK = 32 * 80;
constexpr u32 WS_RC = WS_RA + NWG * RA_BLK;
constexpr u32 RC_BLK = 128 * 128;
constexpr u32 WS_END = WS_RC + NFIN * RC_BLK;
constexpr u32 WS_STAMP = 2u << 20;
static_assert(WS_END <= WS_STAMP, "");
constexpr int AUX_NT = 2, AUX_SC1 = 16;
constexpr u32 SPIN_CAP = 1u << 15;
constexpr u32 OOB = 0x7fffff00u;

#define MEMFENCE() asm volatile("" ::: "memory")
#define SCHED() __builtin_amdgcn_sched_barrier(0)

DI float bf2f_lo(u32 x) { return __builtin_bit_cast(float, x << 16); }
DI float bf2f_hi(u32 x) { return __builtin_bit_cast(float, x & 0xffff0000u); }
typedef float f32x2 __attribute__((ext_vector_type(2)));
DI u32 pk_bf16(float a, float b) { const f32x2 f = {a, b}; return __builtin_bit_cast(u32, __builtin_convertvector(f, bf16x2)); }
DI float bfr(float a) { return bf2f_lo(pk_bf16(a, 0.f)); }
template <int CTRL> DI float dpp(float x) {
  return __builtin_bit_cast(float, __builtin_amdgcn_mov_dpp(__builtin_bit_cast(int, x), CTRL, 0xf, 0xf, true));
}
constexpr int XOR1 = 0xB1, XOR2 = 0x4E, XOR3 = 0x1B, XOR7 = 0x141, XOR8 = 0x128, ROR4 = 0x124;
DI float t32_sum(float u, float v) { auto s = __builtin_amdgcn_permlane32_swap(__float_as_uint(u), __float_as_uint(v), false, false); return __uint_as_float(s[0]) + __uint_as_float(s[1]); }
DI float t16_sum(float u, float v) { auto s = __builtin_amdgcn_permlane16_swap(__float_as_uint(u), __float_as_uint(v), false, false); return __uint_as_float(s[0]) + __uint_as_float(s[1]); }
DI float x16_sum(float x) { return t16_sum(x, x); }
DI float x32_sum(float x) { return t32_sum(x, x); }
DI float x16_max(float x) { auto s = __builtin_amdgcn_permlane16_swap(__float_as_uint(x), __float_as_uint(x), false, false); return fmaxf(__uint_as_float(s[0]), __uint_as_float(s[1])); }
DI float x32_max(float x) { auto s = __builtin_amdgcn_permlane32_swap(__float_as_uint(x), __float_as_uint(x), false, false); return fmaxf(__uint_as_float(s[0]), __uint_as_float(s[1])); }
DI float row_sum(float x) { x += dpp<XOR1>(x); x += dpp<XOR2>(x); x += dpp<XOR7>(x); x += dpp<XOR8>(x); return x; }
DI float wave_sum(float x) { return x32_sum(x16_sum(row_sum(x))); }
template <int CTRL> DI u32 dppu(u32 x) { return (u32)__builtin_amdgcn_mov_dpp((int)x, CTRL, 0xf, 0xf, true); }
DI u32 rotl(u32 x, u32 r) { return __builtin_amdgcn_alignbit(x, x, 32u - r); }
DI void chk(u32& ca, u32& cb, u32 v, u32 i) { ca += v; cb ^= rotl(v, i + 1); }
DI void bar() { asm volatile("s_waitcnt lgkmcnt(0)\n\ts_barrier" ::: "memory"); }
DI float dot2(u32 a, u32 b, float c) { return __builtin_amdgcn_fdot2_f32_bf16(__builtin_bit_cast(bf16x2, a), __builtin_bit_cast(bf16x2, b), c, false); }
DI float ex2(float x) { return __builtin_amdgcn_exp2f(x); }
template <u32 M> DI u32 pdep(u32 x) {
  u32 r = 0, k = 0;
#pragma unroll
  for (int b = 0; b < 12; ++b) if ((M >> b) & 1) { r |= ((x >> k) & 1u) << b; ++k; }
  return r;
}

struct Params {
  const void* x; const void* k; const void* v; const void* wq; const void* wo;
  const void* w1; const void* b1; const void* w2; const void* b2;
  void* out; void* ws;
};

constexpr int L_XN = 0;
constexpr int QSTR = 576;
constexpr int L_Q = L_XN + 2048;
constexpr int L_WO = L_Q + 2 * QSTR;
constexpr int L_QP = L_WO + 64 * 896;
constexpr int L_YM = L_QP + 512;
constexpr int L_RED = L_YM + 4 * 448 * 4;
constexpr int L_ML = L_RED + 8 * 2048;
constexpr int L_O = L_ML + 128;
constexpr int L_FIN = L_O + 128;
constexpr int L_XB = L_FIN + 512;
constexpr int L_EP = L_XB + 64;
constexpr int L_CNT = L_EP + 16;
constexpr int L_END = L_CNT + 64;
enum { C_QPART, C_QRDY, C_HP, C_CHORE, C_TURN, C_PART, C_MERGED };

__global__ __launch_bounds__(512, 2) void fused_decode(Params p) {
  __shared__ __attribute__((aligned(16))) char lds[L_END];
  const int tid = threadIdx.x, lane = tid & 63;
  const int w = __builtin_amdgcn_readfirstlane(tid >> 6);
  const int bid = blockIdx.x;
  const int g = HEADMAP == 0 ? bid & 7 : HEADMAP == 1 ? ((bid >> 6) << 1) | ((bid >> 2) & 1) : bid >> 5;
  const int s = HEADMAP == 0 ? bid >> 3 : HEADMAP == 1 ? (((bid >> 4) & 3) << 3) | ((bid & 3) << 1) | ((bid >> 3) & 1) : bid & 31;
  const int lg = lane >> 4, lc = lane & 15;
#define STAMPC(i)
#define STAMP(i)
#define STAMPB(i, v)
  STAMP(0);
  const auto rW = __builtin_amdgcn_make_buffer_rsrc(p.ws, 0, WS_END, 0x00020000);
  u64 epn = 0;
  u32* const cnt = (u32*)(lds + L_CNT);
  float* const xn = (float*)(lds + L_XN);
  auto lds_arrive = [&](int i) -> u32 {
    asm volatile("s_waitcnt lgkmcnt(0)" ::: "memory");
    u32 r = 0;
    if (lane == 0) r = __hip_atomic_fetch_add(cnt + i, 1u, __ATOMIC_RELAXED, __HIP_MEMORY_SCOPE_WORKGROUP);
    r = __builtin_amdgcn_readfirstlane(r);
    MEMFENCE();
    return r;
  };
  auto lds_wait = [&](int i, u32 v) { while (__hip_atomic_load(cnt + i, __ATOMIC_RELAXED, __HIP_MEMORY_SCOPE_WORKGROUP) != v) __builtin_amdgcn_s_sleep(1); MEMFENCE(); };
  const u64 tstart0 = __builtin_amdgcn_s_memrealtime();
  u64 tstart = tstart0;
  auto wait_until = [&](u32 ticks) { while ((u32)(__builtin_amdgcn_s_memrealtime() - tstart) < ticks) __builtin_amdgcn_s_sleep(1); };
  const bool q_prod = s < 16, has_mlp = s >= 16 && s < (g < 4 ? 24 : 22), cons = s >= 24;
  const int mi = (g < 4 ? g * 8 : 8 + g * 6) + s - 16;
  const int cslot = cons ? (s - 24) * 8 + g : 64 + mi;
  const int fid = (s - 24) * 8 + g;
  const bool fin = cons && fid < NFIN;
  auto gemv32 = [&](const u32x4 (&wr)[7]) {
    float a[8];
#pragma unroll
    for (int e = 0; e < 8; ++e) a[e] = 0.f;
#pragma unroll
    for (int i = 0; i < 7; ++i) { const float xx = xn[16 * (w + 4 * i) + (lane >> 2)];
#pragma unroll
      for (int e = 0; e < 4; ++e) { a[2 * e] = fmaf(xx, bf2f_lo(wr[i][e]), a[2 * e]); a[2 * e + 1] = fmaf(xx, bf2f_hi(wr[i][e]), a[2 * e + 1]); } }
#pragma unroll
    for (int e = 0; e < 4; ++e) a[e] = t32_sum(a[e], a[e + 4]);
#pragma unroll
    for (int e = 0; e < 2; ++e) a[e] = t16_sum(a[e], a[e + 2]);
    const float t1 = a[0] + dpp<XOR8>(a[0]), t2 = a[1] + dpp<XOR8>(a[1]);
    float t = (lane & 8) ? t2 : t1;
    t += dpp<XOR7>(dpp<XOR3>(t));
    if (!(lane & 4)) ((float*)(lds + L_QP))[w * 32 + (lane & 3) * 8 + (lane >> 3)] = t;
  };
  if (PRET && w < 4) {
    const u32x4 z = {0u, 0u, 0u, 0u};
    __builtin_amdgcn_raw_buffer_store_b128(z, rW, tid < 160 ? WS_RA + (g * 32 + s) * RA_BLK + tid * 16 : OOB, 0, AUX_SC1);
    if (PRET > 1 && (cons || has_mlp)) __builtin_amdgcn_raw_buffer_store_b128(z, rW, lane < 56 ? WS_RC + (w * 7 + (lane >> 3)) * RC_BLK + cslot * 128 + (lane & 7) * 16 : OOB, 0, AUX_SC1);
    MEMFENCE();
  }
  u32x4 xr, wq[7];
  u32x2 ep = {0u, 0u};
  if (w < 4 || w == 5) ep = __builtin_amdgcn_raw_buffer_load_b64(rW, WS_EPOCH, 0, AUX_SC1);
  u32 tag_lo = 0, tag_hi = 0;
  auto take_epoch = [&]() {
    asm volatile("" : "+v"(ep));
    const u64 ep64 = ((u64)(u32)__builtin_amdgcn_readfirstlane(ep[1]) << 32) | (u32)__builtin_amdgcn_readfirstlane(ep[0]);
    epn = ep64 * 6364136223846793005ull + 1442695040888963407ull;
    tag_lo = (u32)epn << 16; tag_hi = (u32)(epn >> 16);
  };
  if (w < 4) {
    __builtin_amdgcn_s_setprio(HPRIO);
    const auto rX = __builtin_amdgcn_make_buffer_rsrc((void*)p.x, 0, D * 2, 0x00020000);
    const auto rWq = __builtin_amdgcn_make_buffer_rsrc((void*)p.wq, 0, ((ABL & 8) || !q_prod) ? 0 : D * HQ * DH * 2, 0x00020000);
    xr = __builtin_amdgcn_raw_buffer_load_b128(rX, lane * 16, 0, 0);
    const u32 wqo = (w * 16 + (lane >> 2)) * 8192 + (g * 512 + s * 32) * 2 + (lane & 3) * 16;
#pragma unroll
    for (int i = 0; i < 7; ++i) wq[i] = __builtin_amdgcn_raw_buffer_load_b128(rWq, wqo, i * 64 * 8192, AUX_NT);
  }
  if (w != 4) {
    if (tid < 16) cnt[tid] = 0;
    bar();
  }
  if (__builtin_expect(w < 4, 0)) {
    {
      float xv[8];
#pragma unroll
      for (int e = 0; e < 4; ++e) { xv[2 * e] = bf2f_lo(xr[e]); xv[2 * e + 1] = bf2f_hi(xr[e]); }
      STAMPB(1, xv[0]);
      const float mean = wave_sum(((xv[0] + xv[1]) + (xv[2] + xv[3])) + ((xv[4] + xv[5]) + (xv[6] + xv[7]))) * (1.f / D);
      float sq = 0.f;
#pragma unroll
      for (int e = 0; e < 8; ++e) { const float d = xv[e] - mean; sq = fmaf(d, d, sq); }
      sq = lane < 56 ? sq : 0.f;
      const float var = wave_sum(sq) * (1.f / D);
      const float mb = bfr(mean), den = bfr(__builtin_amdgcn_sqrtf(bfr(bfr(var) + 1e-5f)));
      const float rden = lane < 56 ? __builtin_amdgcn_rcpf(den) : 0.f;
      f32x4 o[2];
#pragma unroll
      for (int e = 0; e < 8; ++e) o[e >> 2][e & 3] = bfr(bfr(xv[e] - mb) * rden);
      *(f32x4*)(xn + lane * 8) = o[0]; *(f32x4*)(xn + lane * 8 + 4) = o[1];
    }
    take_epoch();
    if (q_prod) {
      gemv32(wq);
      STAMPB(2, lane);
      if (lds_arrive(C_QPART) == 3) {
        const float* qp = (const float*)(lds + L_QP) + (lane & 31);
        const float q = (qp[0] + qp[32]) + (qp[64] + qp[96]);
        const u32x2 gr = {tag_lo | (pk_bf16(q, 0.f) & 0xffffu), tag_hi};
        __builtin_amdgcn_raw_buffer_store_b64(gr, rW, lane < 32 ? WS_QG + (g * 512 + s * 32 + lane) * 8 : OOB, 0, AUX_SC1);
      }
    }
    MEMFENCE();
    STAMPB(3, lane);
    __builtin_amdgcn_s_setprio(0);
  }

  float m = -INFINITY, l = 0.f, o[8];
  {
    const auto rK = __builtin_amdgcn_make_buffer_rsrc((void*)p.k, 0, 0x10000000, 0x00020000);
    const auto rV = __builtin_amdgcn_make_buffer_rsrc((void*)p.v, 0, 0x10000000, 0x00020000);
    u32x4 kb[2][8], vb[2][8];
    const u32 koff = (4 * (lc & 3) + (lc >> 2)) * 512 + lg * 16;
    const u32 voff = lg * 512 + lc * 16;
    const u32 tbase = g * (u32)(SK * 512) + (pdep<TMASK>(s) << 13);
    auto toff = [&](u32 i) -> u32 { return tbase + (pdep<~TMASK & 0xfff>(i) << 13); };
    auto loadK = [&](int d, u32 so) {
      u32 oo = koff; asm volatile("" : "+v"(oo));
#pragma unroll
      for (int j = 0; j < 8; ++j) kb[d][j] = __builtin_amdgcn_raw_buffer_load_b128(rK, oo + j * 64, so, KVAUX);
      MEMFENCE(); SCHED();
    };
    auto loadV = [&](int d, u32 so) {
      u32 oo = voff; asm volatile("" : "+v"(oo));
#pragma unroll
      for (int j = 0; j < 8; ++j) vb[d][j] = __builtin_amdgcn_raw_buffer_load_b128(rV, oo + ((j >> 1) & 1) * 2048 + (j & 1) * 256, so + (j >> 2) * 4096, KVAUX);
      MEMFENCE(); SCHED();
    };
    const u32 pair0 = w ^ 4;
    {
      const u32 so0 = toff(2 * pair0), so1 = toff(2 * pair0 + 1);
      asm volatile("" :: "s"(so0), "s"(so1), "v"(koff), "v"(voff));
      if (pair0) lds_wait(C_TURN, pair0); else if (KVT && q_prod) wait_until(KVT);
      STAMPC(1);
      if (w >= 4) STAMPB(2, lane);
      loadK(0, so0);
      if (w == 4) bar();
      loadV(0, so0); loadK(1, so1); loadV(1, so1);
      if (lane == 0) __hip_atomic_store(cnt + C_TURN, pair0 + 1, __ATOMIC_RELAXED, __HIP_MEMORY_SCOPE_WORKGROUP);
      MEMFENCE();
    }
    STAMPC(2);
    if (w >= 4) STAMPB(1, lane);
    if (w == 5) {
      take_epoch();
      if (!(VAR & 8)) {
        const u32 qgoff = WS_QG + (g * 512 + lane * 2) * 8;
        u32x4 q[4];
        bool ok[4] = {false, false, false, false};
        wait_until(QT);
        for (u32 spins = 0; spins < SPIN_CAP; ++spins) {
          MEMFENCE();
#pragma unroll
          for (int i = 0; i < 4; ++i) if (!ok[i]) q[i] = __builtin_amdgcn_raw_buffer_load_b128(rW, qgoff + i * 1024, 0, AUX_SC1);
#pragma unroll
          for (int i = 0; i < 4; ++i) ok[i] = ((q[i][0] & 0xffff0000u) == tag_lo) & (q[i][1] == tag_hi) & ((q[i][2] & 0xffff0000u) == tag_lo) & (q[i][3] == tag_hi);
          if (__all(ok[0] & ok[1] & ok[2] & ok[3])) break;
        }
#pragma unroll
        for (int i = 0; i < 4; ++i) *(u32*)(lds + L_Q + (i >> 1) * QSTR + (i & 1) * 256 + lane * 4) = (q[i][0] & 0xffffu) | (q[i][2] << 16);
      }
      if (lane == 0) *(u64*)(lds + L_EP) = epn;
      lds_arrive(C_QRDY);
      STAMPB(4, lane);
    }
    lds_wait(C_QRDY, 1);
    if (w == 4) lds_wait(C_TURN, 8);
    STAMP(1); STAMPB(5, lane);
    bf16x8 qf[8];
#pragma unroll
    for (int j = 0; j < 8; ++j) qf[j] = *(const bf16x8*)(lds + L_Q + (lc & 1) * QSTR + lg * 16 + j * 64);
    constexpr float C2 = 0.0625f * 1.4426950408889634f;
    f32x4 acc[16];
#pragma unroll
    for (int i = 0; i < 16; ++i) acc[i] = f32x4{0.f, 0.f, 0.f, 0.f};
    auto qk = [&](int d) -> f32x4 {
      f32x4 sc = {0.f, 0.f, 0.f, 0.f};
#pragma unroll
      for (int j = 0; j < 8; ++j) sc = __builtin_amdgcn_mfma_f32_16x16x32_bf16(__builtin_bit_cast(bf16x8, kb[d][j]), qf[j], sc, 0, 0, 0);
      asm volatile("" : "+v"(sc) :: "memory"); SCHED();
      return sc;
    };
    auto pv = [&](int d, f32x4 sc) {
      const u32 sa = pk_bf16(sc[0], sc[1]), sb = pk_bf16(sc[2], sc[3]);
      const float t0 = bf2f_lo(sa), t1 = bf2f_hi(sa), t2 = bf2f_lo(sb), t3 = bf2f_hi(sb);
      const float tl = fmaxf(fmaxf(t0, t1), fmaxf(t2, t3)) * C2;
      if (__any(tl > m)) {
        const float mn = fmaxf(m, x32_max(x16_max(tl))), al = ex2(m - mn);
        m = mn; l *= al;
        const float a0 = dpp<0xA0>(al), a1 = dpp<0xF5>(al);
#pragma unroll
        for (int i = 0; i < 16; ++i) { acc[i][0] *= a0; acc[i][1] *= a1; }
      }
      const float p0 = ex2(fmaf(t0, C2, -m)), p1 = ex2(fmaf(t1, C2, -m)), p2 = ex2(fmaf(t2, C2, -m)), p3 = ex2(fmaf(t3, C2, -m));
      l += (p0 + p1) + (p2 + p3);
      const u32x2 pp = {pk_bf16(p0, p1), pk_bf16(p2, p3)};
      const s16x4 pf = __builtin_bit_cast(s16x4, pp);
#pragma unroll
      for (int hf = 0; hf < 2; ++hf)
#pragma unroll
        for (int e = 0; e < 8; ++e) {
          const u32 sel = (e & 1) ? 0x07060302u : 0x05040100u;
          const u32x2 bb = {__builtin_amdgcn_perm(vb[d][2 + hf][e >> 1], vb[d][hf][e >> 1], sel), __builtin_amdgcn_perm(vb[d][6 + hf][e >> 1], vb[d][4 + hf][e >> 1], sel)};
          acc[hf * 8 + e] = __builtin_amdgcn_mfma_f32_16x16x16bf16_1k(pf, __builtin_bit_cast(s16x4, bb), acc[hf * 8 + e], 0, 0, 0);
        }
      MEMFENCE(); SCHED();
    };
    if (w == 4) {
      for (u32 tp = 8; tp < NPAIR; ++tp) {
#pragma unroll
        for (int d = 0; d < 2; ++d) {
          const u32 so = toff(2 * tp + d);
          const f32x4 sc = qk(d);
          loadK(d, so); pv(d, sc); loadV(d, so);
        }
      }
    }
#pragma unroll
    for (int d = 0; d < 2; ++d) { const f32x4 sc = qk(d); pv(d, sc); }
    STAMP(2);
    l = x32_sum(x16_sum(l));
#pragma unroll
    for (int e = 0; e < 8; ++e) { const float lo = (lg & 1) ? acc[e][1] : acc[e][0], hi = (lg & 1) ? acc[8 + e][1] : acc[8 + e][0]; o[e] = (lg >> 1) ? hi : lo; }
  }
  const int ridx = (lg & 1) * 256 + (lg >> 1) * 128 + lc * 8;
  if (w != 4) {
    float* red = (float*)(lds + L_RED) + ridx;
    float* ml = (float*)(lds + L_ML);
    *(f32x4*)(red + w * 512) = f32x4{o[0], o[1], o[2], o[3]}; *(f32x4*)(red + w * 512 + 4) = f32x4{o[4], o[5], o[6], o[7]};
    if (lane < 2) *(float2*)(ml + w * 4 + lane * 2) = make_float2(m, l);
    if (lds_arrive(C_PART) == 6) {
      const int h2 = (lg & 1) * 2;
      float M = -INFINITY, L = 0.f;
      f32x4 a0 = {0.f, 0.f, 0.f, 0.f}, a1 = a0;
#pragma unroll
      for (int i = 0; i < 8; ++i) if (i != 4) M = fmaxf(M, ml[i * 4 + h2]);
#pragma unroll
      for (int i = 0; i < 8; ++i) if (i != 4) { const float e = ex2(ml[i * 4 + h2] - M);
        L = fmaf(ml[i * 4 + h2 + 1], e, L); a0 += *(const f32x4*)(red + i * 512) * e; a1 += *(const f32x4*)(red + i * 512 + 4) * e; }
      *(f32x4*)(red + 4 * 512) = a0; *(f32x4*)(red + 4 * 512 + 4) = a1;
      if (lc == 0 && lg < 2) *(float2*)(ml + 16 + h2) = make_float2(M, L);
      lds_arrive(C_MERGED);
    }
  } else {
    lds_wait(C_MERGED, 1);
    const float* red = (const float*)(lds + L_RED) + 4 * 512 + ridx;
    const f32x4 ma = *(const f32x4*)((const float*)(lds + L_ML) + 16);
    const f32x4 a0 = *(const f32x4*)red, a1 = *(const f32x4*)(red + 4);
    const float m0 = dpp<0xA0>(m), m1 = dpp<0xF5>(m), l0 = dpp<0xA0>(l), l1 = dpp<0xF5>(l);
    const float mh = (lg & 1) ? m1 : m0, lh = (lg & 1) ? l1 : l0, Ma = (lg & 1) ? ma[2] : ma[0], La = (lg & 1) ? ma[3] : ma[1];
    const float M = fmaxf(mh, Ma), e0 = ex2(mh - M), e1 = ex2(Ma - M);
    const u32 rec = WS_RA + (g * 32 + s) * RA_BLK + ((lg & 1) * 16 + (lg >> 1) * 8 + (lc >> 1)) * 80;
    const u32x2 ep2 = *(const u32x2*)(lds + L_EP);
    u32x4 v0, v1;
#pragma unroll
    for (int e = 0; e < 4; ++e) { v0[e] = __float_as_uint(fmaf(o[e], e0, a0[e] * e1)); v1[e] = __float_as_uint(fmaf(o[4 + e], e0, a1[e] * e1)); }
    const u32 Mb = __float_as_uint(M), Lb = __float_as_uint(fmaf(lh, e0, La * e1));
    STAMP(3);
    if (VAR & 8192) wait_until(DLY);
    if (!(VAR & 32)) {
    __builtin_amdgcn_raw_buffer_store_b128(v0, rW, rec + (lc & 1) * 32, 0, AUX_SC1);
    __builtin_amdgcn_raw_buffer_store_b128(v1, rW, rec + (lc & 1) * 32 + 16, 0, AUX_SC1);
    u32 ca = 0, cb = 0;
#pragma unroll
    for (int e = 0; e < 4; ++e) { chk(ca, cb, v0[e], (lc & 1) * 8 + e); chk(ca, cb, v1[e], (lc & 1) * 8 + 4 + e); }
    ca += dppu<XOR1>(ca); cb ^= dppu<XOR1>(cb);
    ca += ep2[0]; cb ^= ep2[1];
    chk(ca, cb, Mb, 16); chk(ca, cb, Lb, 17);
    __builtin_amdgcn_raw_buffer_store_b128(u32x4{Mb, Lb, ca, cb}, rW, (lc & 1) ? OOB : rec + 64, 0, AUX_SC1);
    }
    MEMFENCE();
  }

  auto publish_c = [&](float y0, float y1) {
    const u32 b0 = __float_as_uint(y0), b1 = __float_as_uint(y1);
    u32 ca = 0, cb = 0;
    chk(ca, cb, b0, (lane & 7) * 2); chk(ca, cb, b1, (lane & 7) * 2 + 1);
    ca += dppu<XOR1>(ca); cb ^= dppu<XOR1>(cb); ca += dppu<XOR2>(ca); cb ^= dppu<XOR2>(cb); ca += dppu<XOR7>(ca); cb ^= dppu<XOR7>(cb);
    ca += (u32)epn; cb ^= (u32)(epn >> 32);
    const u32 z0 = dppu<XOR1>(b0), z1 = dppu<XOR1>(b1);
    const u32 rec = WS_RC + (w * 7 + (lane >> 3)) * RC_BLK + cslot * 128;
    const bool odd = lane & 1;
    const u32x4 ov = {odd ? ca : b0, odd ? cb : b1, odd ? ca : z0, odd ? cb : z1};
    __builtin_amdgcn_raw_buffer_store_b128(ov, rW, lane >= 56 ? OOB : !odd ? rec + (lane & 7) * 8 : (lane & 7) == 1 ? rec + 64 : OOB, 0, AUX_SC1);
    MEMFENCE();
  };
  auto gather_c = [&](int slot0, int nslot, u32 e0, u32 e1) {
    const u32 off = WS_RC + fid * RC_BLK + (slot0 + (lane >> 2)) * 128;
    u32x4 yv = {0u, 0u, 0u, 0u}; u32x2 cv;
    bool ok = (lane >> 2) >= nslot;
    for (u32 spins = 0; spins < SPIN_CAP; ++spins) {
      MEMFENCE();
      if (!ok) {
        yv = __builtin_amdgcn_raw_buffer_load_b128(rW, off + (lane & 3) * 16, 0, AUX_SC1);
        cv = __builtin_amdgcn_raw_buffer_load_b64(rW, off + 64, 0, AUX_SC1);
        u32 ca = 0, cb = 0;
#pragma unroll
        for (int e = 0; e < 4; ++e) chk(ca, cb, yv[e], (lane & 3) * 4 + e);
        ca += dppu<XOR1>(ca); cb ^= dppu<XOR1>(cb); ca += dppu<XOR2>(ca); cb ^= dppu<XOR2>(cb);
        u32 bad = ((ca + e0) ^ cv[0]) | ((cb ^ e1) ^ cv[1]);
        bad |= dppu<XOR1>(bad); bad |= dppu<XOR2>(bad);
        ok = bad == 0;
      }
      const int miss = __popcll(__ballot(!ok));
      if (miss == 0) break;
      for (int i = 8; i < miss; i += 8) __builtin_amdgcn_s_sleep(CSLEEP);
    }
    const float v0 = t32_sum(__uint_as_float(yv[0]), __uint_as_float(yv[2])), v1 = t32_sum(__uint_as_float(yv[1]), __uint_as_float(yv[3]));
    float t = t16_sum(v0, v1);
    t += dpp<ROR4>(t); t += dpp<XOR8>(t);
    if (!(lane & 12)) ((float*)(lds + L_FIN))[(w ^ 4) * 16 + (lane & 3) * 4 + lg] = t;
  };

  u32 wp[64];
  if (w < 4 && (has_mlp || cons)) {
    const u32 rowoff = lane < 56 ? lane * 16 : OOB;
    const int l2 = w * 56 + (lane < 56 ? lane : 0);
    if (CHT) wait_until(1200 + (((s - 16) & ~1) * 4 + (cons ? (s & 1) * 4 : 0) + w) * CHT);
    if (has_mlp) {
      const auto rW1 = __builtin_amdgcn_make_buffer_rsrc((void*)p.w1, 0, (ABL & 1) ? 0 : D * FF * 2, 0x00020000);
      const auto rW2 = __builtin_amdgcn_make_buffer_rsrc((void*)p.w2, 0, (ABL & 2) ? 0 : D * FF * 2, 0x00020000);
      const auto rB1 = __builtin_amdgcn_make_buffer_rsrc((void*)p.b1, 0, FF * 2, 0x00020000);
      const int u0 = mi * 32;
      u32x4 w1r[7], w2r[8];
#pragma unroll
      for (int i = 0; i < 7; ++i) w1r[i] = __builtin_amdgcn_raw_buffer_load_b128(rW1, (w * 16 + (lane >> 2)) * (FF * 2) + u0 * 2 + (lane & 3) * 16, i * 64 * (FF * 2), AUX_NT);
#pragma unroll
      for (int i = 0; i < 8; ++i) w2r[i] = __builtin_amdgcn_raw_buffer_load_b128(rW2, rowoff, (u0 + w * 8 + i) * 896, AUX_NT);
      const u32 b1r = __builtin_amdgcn_raw_buffer_load_b16(rB1, (u0 + w * 8 + (lane & 7)) * 2, 0, 0);
      gemv32(w1r);
      lds_arrive(C_HP);
      lds_wait(C_HP, 4);
      const float* hp = (const float*)(lds + L_QP) + w * 8 + (lane & 7);
      float hv = bfr(bfr((hp[0] + hp[32]) + (hp[64] + hp[96])) + bf2f_lo(b1r));
      hv = bfr(0.5f * hv * (1.f + tanhf(0.7978845608028654f * (hv + 0.044715f * hv * hv * hv))));
      float ym[8];
#pragma unroll
      for (int e = 0; e < 8; ++e) ym[e] = 0.f;
#pragma unroll
      for (int r = 0; r < 8; ++r) {
        const float hr = __builtin_bit_cast(float, __builtin_amdgcn_readlane(__builtin_bit_cast(int, hv), r));
#pragma unroll
        for (int e = 0; e < 4; ++e) { ym[2 * e] = fmaf(hr, bf2f_lo(w2r[r][e]), ym[2 * e]); ym[2 * e + 1] = fmaf(hr, bf2f_hi(w2r[r][e]), ym[2 * e + 1]); }
      }
      if (lane < 56) { float* y = (float*)(lds + L_YM) + w * 448 + lane * 8;
        *(f32x4*)y = f32x4{ym[0], ym[1], ym[2], ym[3]}; *(f32x4*)(y + 4) = f32x4{ym[4], ym[5], ym[6], ym[7]}; }
      lds_arrive(C_CHORE);
      lds_wait(C_CHORE, 4);
      const float2* y = (const float2*)(lds + L_YM) + l2;
      const float2 a = y[0], b = y[224], c = y[448], d = y[672];
      if (!(VAR & 64)) publish_c((a.x + b.x) + (c.x + d.x), (a.y + b.y) + (c.y + d.y));
    } else {
      const auto rWo = __builtin_amdgcn_make_buffer_rsrc((void*)p.wo, 0, (ABL & 4) ? 0 : D * HQ * DH * 2, 0x00020000);
      u32x4 wor[16];
#pragma unroll
      for (int i = 0; i < 16; ++i) wor[i] = __builtin_amdgcn_raw_buffer_load_b128(rWo, rowoff, (g * 512 + (s - 24) * 64 + w * 16 + i) * 896, AUX_NT);
      if (lane < 56) {
#pragma unroll
        for (int i = 0; i < 16; ++i) *(u32x4*)(lds + L_WO + (w * 16 + i) * 896 + lane * 16) = wor[i];
      }
      lds_arrive(C_CHORE);
      if (fin) {
        if (tid < 16) { const int col = fid * 16 + tid;
          ((float*)(lds + L_XB))[tid] = __bfloat162float(((const __hip_bfloat16*)p.x)[col]) + __bfloat162float(((const __hip_bfloat16*)p.b2)[col]); }
        wait_until(1200 + 64 * CHT + 400);
        if (!(VAR & (32 | 2048))) gather_c(64 + w * 16, w < 3 ? 16 : 8, (u32)epn, (u32)(epn >> 32));
      }
      lds_wait(C_CHORE, 4);
#pragma unroll
      for (int k = 0; k < 32; ++k) {
        const u32 a = ((const u32*)(lds + L_WO))[(2 * k) * 224 + l2], b = ((const u32*)(lds + L_WO))[(2 * k + 1) * 224 + l2];
        wp[2 * k] = __builtin_amdgcn_perm(b, a, 0x05040100u); wp[2 * k + 1] = __builtin_amdgcn_perm(b, a, 0x07060302u);
      }
      STAMPB(6, wp[63]);
    }
  }
  if (cons && !(VAR & (32 | 2048))) {
    bar();
    const u32x2 ep2 = *(const u32x2*)(lds + L_EP);
    if (w >= 4) {
      const u32 roff = WS_RA + (g * 32 + (lane >> 2)) * RA_BLK + ((s - 24) * 4 + w - 4) * 80;
      u32x4 ov[2], mv[2];
      bool ok[2] = {false, false};
      for (u32 spins = 0; spins < SPIN_CAP; ++spins) {
        MEMFENCE();
#pragma unroll
        for (int i = 0; i < 2; ++i) if (!ok[i]) {
          ov[i] = __builtin_amdgcn_raw_buffer_load_b128(rW, roff + i * 16 * RA_BLK + (lane & 3) * 16, 0, AUX_SC1);
          mv[i] = __builtin_amdgcn_raw_buffer_load_b128(rW, roff + i * 16 * RA_BLK + 64, 0, AUX_SC1);
        }
        int miss = 0;
#pragma unroll
        for (int i = 0; i < 2; ++i) {
          u32 ca = 0, cb = 0;
#pragma unroll
          for (int e = 0; e < 4; ++e) chk(ca, cb, ov[i][e], (lane & 3) * 4 + e);
          ca += dppu<XOR1>(ca); cb ^= dppu<XOR1>(cb); ca += dppu<XOR2>(ca); cb ^= dppu<XOR2>(cb);
          ca += ep2[0]; cb ^= ep2[1];
          chk(ca, cb, mv[i][0], 16); chk(ca, cb, mv[i][1], 17);
          u32 bad = (ca ^ mv[i][2]) | (cb ^ mv[i][3]);
          bad |= dppu<XOR1>(bad); bad |= dppu<XOR2>(bad);
          ok[i] = bad == 0;
          miss += __popcll(__ballot(!ok[i]));
        }
        if (miss == 0) break;
        for (int i = PTHR; i < miss; i += 4) __builtin_amdgcn_s_sleep(PSLEEP);
      }
      const float m0 = __uint_as_float(mv[0][0]), m1 = __uint_as_float(mv[1][0]);
      float M = fmaxf(m0, m1);
      M = fmaxf(M, dpp<ROR4>(M)); M = fmaxf(M, dpp<XOR8>(M)); M = x32_max(x16_max(M));
      const float w0 = ex2(m0 - M), w1 = ex2(m1 - M);
      float L = fmaf(w0, __uint_as_float(mv[0][1]), w1 * __uint_as_float(mv[1][1]));
      L += dpp<ROR4>(L); L += dpp<XOR8>(L); L = x32_sum(x16_sum(L));
      float oo[4];
#pragma unroll
      for (int e = 0; e < 4; ++e) oo[e] = fmaf(w0, __uint_as_float(ov[0][e]), w1 * __uint_as_float(ov[1][e]));
      oo[0] = t32_sum(oo[0], oo[2]); oo[1] = t32_sum(oo[1], oo[3]);
      float t = t16_sum(oo[0], oo[1]);
      t += dpp<ROR4>(t); t += dpp<XOR8>(t);
      if (!(lane & 12)) ((unsigned short*)(lds + L_O))[(w - 4) * 16 + (lane & 3) * 4 + lg] = (unsigned short)pk_bf16(t * __builtin_amdgcn_rcpf(L), 0.f);
    }
    bar();
    STAMP(5);
    if (w < 4) {
      float y0 = 0.f, y1 = 0.f, y2 = 0.f, y3 = 0.f;
#pragma unroll
      for (int k4 = 0; k4 < 8; ++k4) { const u32x4 op = ((const u32x4*)(lds + L_O))[k4];
#pragma unroll
        for (int e = 0; e < 4; e += 2) {
          y0 = dot2(wp[8 * k4 + 2 * e], op[e], y0); y1 = dot2(wp[8 * k4 + 2 * e + 1], op[e], y1);
          y2 = dot2(wp[8 * k4 + 2 * e + 2], op[e + 1], y2); y3 = dot2(wp[8 * k4 + 2 * e + 3], op[e + 1], y3);
        } }
      publish_c(y0 + y2, y1 + y3);
    }
    STAMP(6);
    if (fin) {
      if (w >= 4) gather_c((w - 4) * 16, 16, ep2[0], ep2[1]);
      bar();
      if (tid < 16) {
        const float* f = (const float*)(lds + L_FIN) + tid;
        const float r = (((const float*)(lds + L_XB))[tid] + ((f[64] + f[80]) + (f[96] + f[112]))) + ((f[0] + f[16]) + (f[32] + f[48]));
        ((unsigned short*)p.out)[fid * 16 + tid] = (unsigned short)pk_bf16(r, 0.f);
        if (fid == 0 && tid == 0) __builtin_amdgcn_raw_buffer_store_b64(ep2, rW, WS_EPOCH, 0, AUX_SC1);
      }
    }
  }
  if ((VAR & (32 | 2048)) && cons && w < 4 && wp[5] == 0x12345u) ((float*)p.ws)[77] = 1.f;
  if (bid == 0 && w == 7) { while ((u32)(__builtin_amdgcn_s_memrealtime() - tstart0) < 12000) __builtin_amdgcn_s_sleep(4); }
}

extern "C" void kernel_launch(void* const* d_in, const int* in_sizes, int n_in, void* d_out, int out_size, void* d_ws, size_t ws_size, hipStream_t stream) {
  Params p;
  p.x = d_in[0]; p.k = d_in[1]; p.v = d_in[2]; p.wq = d_in[3]; p.wo = d_in[4];
  p.w1 = d_in[6]; p.b1 = d_in[7]; p.w2 = d_in[8]; p.b2 = d_in[9];
  p.out = d_out; p.ws = d_ws;
  hipLaunchKernelGGL(fused_decode, dim3(NWG), dim3(512), 0, stream, p);
}
```

```cpp
#include <hip/hip_runtime.h>
#include <hip/hip_bf16.h>
#include <stdint.h>

typedef unsigned u32;
typedef unsigned long long u64;
typedef u32 u32x4 __attribute__((ext_vector_type(4)));
typedef u32 u32x2 __attribute__((ext_vector_type(2)));
typedef float f32x4 __attribute__((ext_vector_type(4)));
typedef __bf16 bf16x8 __attribute__((ext_vector_type(8)));
typedef __bf16 bf16x2 __attribute__((ext_vector_type(2)));
typedef short s16x4 __attribute__((ext_vector_type(4)));
#define DI __device__ __forceinline__

constexpr u32 TMASK = 0x178;
constexpr u32 T_QPOLL = 450, T_CHORE = 1200, T_STAG = 20;

constexpr int D = 448, HQ = 16, SK = 65536, DH = 256, FF = 1792;
constexpr int NWG = 256;
constexpr int NPAIR = 64;
constexpr int NFIN = 28;
constexpr u32 WS_EPOCH = 0;
constexpr u32 WS_QG = 4096;
constexpr u32 WS_RA = WS_QG + 8 * 512 * 8;
constexpr u32 RA_BLK = 32 * 80;
constexpr u32 WS_RC = WS_RA + NWG * RA_BLK;
constexpr u32 RC_BLK = 128 * 128;
constexpr u32 WS_END = WS_RC + NFIN * RC_BLK;
constexpr int AUX_NT = 2, AUX_SC1 = 16;
constexpr float NEG = -3.0e38f;
constexpr u32 SPIN_CAP = 1u << 15;
constexpr u32 OOB = 0x7fffff00u;

#define MEMFENCE() asm volatile("" ::: "memory")
#define SCHED() __builtin_amdgcn_sched_barrier(0)

DI float bf2f_lo(u32 x) { return __builtin_bit_cast(float, x << 16); }
DI float bf2f_hi(u32 x) { return __builtin_bit_cast(float, x & 0xffff0000u); }
typedef float f32x2 __attribute__((ext_vector_type(2)));
DI u32 pk_bf16(float a, float b) { const f32x2 f = {a, b}; return __builtin_bit_cast(u32, __builtin_convertvector(f, bf16x2)); }
DI float bfr(float a) { return bf2f_lo(pk_bf16(a, 0.f)); }
template <int CTRL> DI float dpp(float x) {
  return __builtin_bit_cast(float, __builtin_amdgcn_mov_dpp(__builtin_bit_cast(int, x), CTRL, 0xf, 0xf, true));
}
constexpr int XOR1 = 0xB1, XOR2 = 0x4E, XOR3 = 0x1B, XOR7 = 0x141, XOR8 = 0x128, ROR4 = 0x124;
DI float t32_sum(float u, float v) { auto s = __builtin_amdgcn_permlane32_swap(__float_as_uint(u), __float_as_uint(v), false, false); return __uint_as_float(s[0]) + __uint_as_float(s[1]); }
DI float t16_sum(float u, float v) { auto s = __builtin_amdgcn_permlane16_swap(__float_as_uint(u), __float_as_uint(v), false, false); return __uint_as_float(s[0]) + __uint_as_float(s[1]); }
DI float x16_sum(float x) { return t16_sum(x, x); }
DI float x32_sum(float x) { return t32_sum(x, x); }
DI float x16_max(float x) { auto s = __builtin_amdgcn_permlane16_swap(__float_as_uint(x), __float_as_uint(x), false, false); return fmaxf(__uint_as_float(s[0]), __uint_as_float(s[1])); }
DI float x32_max(float x) { auto s = __builtin_amdgcn_permlane32_swap(__float_as_uint(x), __float_as_uint(x), false, false); return fmaxf(__uint_as_float(s[0]), __uint_as_float(s[1])); }
DI float row_sum(float x) { x += dpp<XOR1>(x); x += dpp<XOR2>(x); x += dpp<XOR7>(x); x += dpp<XOR8>(x); return x; }
DI float wave_sum(float x) { return x32_sum(x16_sum(row_sum(x))); }
template <int CTRL> DI u32 dppu(u32 x) { return (u32)__builtin_amdgcn_mov_dpp((int)x, CTRL, 0xf, 0xf, true); }
DI u32 rotl(u32 x, u32 r) { return __builtin_amdgcn_alignbit(x, x, 32u - r); }
DI void chk(u32& ca, u32& cb, u32 v, u32 i) { ca += v; cb ^= rotl(v, i + 1); }
DI void bar() { asm volatile("s_waitcnt lgkmcnt(0)\n\ts_barrier" ::: "memory"); }
DI float dot2(u32 a, u32 b, float c) { return __builtin_amdgcn_fdot2_f32_bf16(__builtin_bit_cast(bf16x2, a), __builtin_bit_cast(bf16x2, b), c, false); }
DI float ex2(float x) { return __builtin_amdgcn_exp2f(x); }
template <u32 M> DI u32 pdep(u32 x) {
  u32 r = 0, k = 0;
#pragma unroll
  for (int b = 0; b < 12; ++b) if ((M >> b) & 1) { r |= ((x >> k) & 1u) << b; ++k; }
  return r;
}

struct Params {
  const void* x; const void* k; const void* v; const void* wq; const void* wo;
  const void* w1; const void* b1; const void* w2; const void* b2;
  void* out; void* ws;
};

constexpr int L_XN = 0;
constexpr int QSTR = 576;
constexpr int L_Q = L_XN + 2048;
constexpr int L_WO = L_Q + 2 * QSTR;
constexpr int L_QP = L_WO + 64 * 896;
constexpr int L_YM = L_QP + 512;
constexpr int L_RED = L_YM + 4 * 448 * 4;
constexpr int L_ML = L_RED + 8 * 2048;
constexpr int L_O = L_ML + 128;
constexpr int L_FIN = L_O + 128;
constexpr int L_XB = L_FIN + 512;
constexpr int L_EP = L_XB + 64;
constexpr int L_CNT = L_EP + 16;
constexpr int L_END = L_CNT + 64;
enum { C_QPART, C_QRDY, C_HP, C_CHORE, C_TURN, C_PART, C_MERGED, C_END };

__global__ __launch_bounds__(512, 2) void fused_decode(Params p) {
  __shared__ __attribute__((aligned(16))) char lds[L_END];
  const int tid = threadIdx.x, lane = tid & 63;
  const int w = __builtin_amdgcn_readfirstlane(tid >> 6);
  const int bid = blockIdx.x;
  const int g = ((bid >> 6) << 1) | ((bid >> 2) & 1);
  const int s = (((bid >> 4) & 3) << 3) | ((bid & 3) << 1) | ((bid >> 3) & 1);
  const int lg = lane >> 4, lc = lane & 15;
  if (w == 4) __builtin_amdgcn_s_setprio(3);
  const auto rW = __builtin_amdgcn_make_buffer_rsrc(p.ws, 0, WS_END, 0x00020000);
  u64 epn = 0;
  u32* const cnt = (u32*)(lds + L_CNT);
  float* const xn = (float*)(lds + L_XN);
  auto lds_arrive = [&](int i) -> u32 {
    asm volatile("s_waitcnt lgkmcnt(0)" ::: "memory");
    u32 r = 0;
    if (lane == 0) r = __hip_atomic_fetch_add(cnt + i, 1u, __ATOMIC_RELAXED, __HIP_MEMORY_SCOPE_WORKGROUP);
    r = __builtin_amdgcn_readfirstlane(r);
    MEMFENCE();
    return r;
  };
  auto lds_wait = [&](int i, u32 v) { while (__hip_atomic_load(cnt + i, __ATOMIC_RELAXED, __HIP_MEMORY_SCOPE_WORKGROUP) != v) __builtin_amdgcn_s_sleep(1); MEMFENCE(); };
  const u64 tstart = __builtin_amdgcn_s_memrealtime();
  auto wait_until = [&](u32 ticks) { while ((u32)(__builtin_amdgcn_s_memrealtime() - tstart) < ticks) __builtin_amdgcn_s_sleep(1); };
  const bool q_prod = s < 16, has_mlp = s >= 16 && s < (g < 4 ? 24 : 22), cons = s >= 24;
  const int mi = (g < 4 ? g * 8 : 8 + g * 6) + s - 16;
  const int cslot = cons ? (s - 24) * 8 + g : 64 + mi;
  const int fid = (s - 24) * 8 + g;
  const bool fin = cons && fid < NFIN;
  auto gemv32 = [&](const u32x4 (&wr)[7]) {
    float a[8];
#pragma unroll
    for (int e = 0; e < 8; ++e) a[e] = 0.f;
#pragma unroll
    for (int i = 0; i < 7; ++i) { const float xx = xn[16 * (w + 4 * i) + (lane >> 2)];
#pragma unroll
      for (int e = 0; e < 4; ++e) { a[2 * e] = fmaf(xx, bf2f_lo(wr[i][e]), a[2 * e]); a[2 * e + 1] = fmaf(xx, bf2f_hi(wr[i][e]), a[2 * e + 1]); } }
#pragma unroll
    for (int e = 0; e < 4; ++e) a[e] = t32_sum(a[e], a[e + 4]);
#pragma unroll
    for (int e = 0; e < 2; ++e) a[e] = t16_sum(a[e], a[e + 2]);
    const float t1 = a[0] + dpp<XOR8>(a[0]), t2 = a[1] + dpp<XOR8>(a[1]);
    float t = (lane & 8) ? t2 : t1;
    t += dpp<XOR7>(dpp<XOR3>(t));
    if (!(lane & 4)) ((float*)(lds + L_QP))[w * 32 + (lane & 3) * 8 + (lane >> 3)] = t;
  };
  u32x4 xr, wq[7];
  u32x2 ep = {0u, 0u};
  if (w < 4 || w == 5) ep = __builtin_amdgcn_raw_buffer_load_b64(rW, WS_EPOCH, 0, AUX_SC1);
  u32 tag_lo = 0, tag_hi = 0;
  auto take_epoch = [&]() {
    asm volatile("" : "+v"(ep));
    const u64 ep64 = ((u64)(u32)__builtin_amdgcn_readfirstlane(ep[1]) << 32) | (u32)__builtin_amdgcn_readfirstlane(ep[0]);
    epn = ep64 * 6364136223846793005ull + 1442695040888963407ull;
    tag_lo = (u32)epn << 16; tag_hi = (u32)(epn >> 16);
  };
  if (w < 4) {
    __builtin_amdgcn_s_setprio(2);
    const auto rX = __builtin_amdgcn_make_buffer_rsrc((void*)p.x, 0, (q_prod || has_mlp) ? D * 2 : 0, 0x00020000);
    const auto rWq = __builtin_amdgcn_make_buffer_rsrc((void*)p.wq, 0, q_prod ? D * HQ * DH * 2 : 0, 0x00020000);
    xr = __builtin_amdgcn_raw_buffer_load_b128(rX, lane * 16, 0, 0);
    const u32 wqo = (w * 16 + (lane >> 2)) * 8192 + (g * 512 + s * 32) * 2 + (lane & 3) * 16;
#pragma unroll
    for (int i = 0; i < 7; ++i) wq[i] = __builtin_amdgcn_raw_buffer_load_b128(rWq, wqo, i * 64 * 8192, AUX_NT);
  }
  if (w != 4) {
    if (tid < 16) cnt[tid] = 0;
    bar();
  }
  if (__builtin_expect(w < 4, 0)) {
    {
      float xv[8];
#pragma unroll
      for (int e = 0; e < 4; ++e) { xv[2 * e] = bf2f_lo(xr[e]); xv[2 * e + 1] = bf2f_hi(xr[e]); }
      const float mean = wave_sum(((xv[0] + xv[1]) + (xv[2] + xv[3])) + ((xv[4] + xv[5]) + (xv[6] + xv[7]))) * (1.f / D);
      float sq = 0.f;
#pragma unroll
      for (int e = 0; e < 8; ++e) { const float d = xv[e] - mean; sq = fmaf(d, d, sq); }
      sq = lane < 56 ? sq : 0.f;
      const float var = wave_sum(sq) * (1.f / D);
      const float mb = bfr(mean), den = bfr(__builtin_amdgcn_sqrtf(bfr(bfr(var) + 1e-5f)));
      const float rden = lane < 56 ? __builtin_amdgcn_rcpf(den) : 0.f;
      f32x4 o[2];
#pragma unroll
      for (int e = 0; e < 8; ++e) o[e >> 2][e & 3] = bfr(bfr(xv[e] - mb) * rden);
      *(f32x4*)(xn + lane * 8) = o[0]; *(f32x4*)(xn + lane * 8 + 4) = o[1];
    }
    take_epoch();
    if (q_prod) {
      gemv32(wq);
      if (lds_arrive(C_QPART) == 3) {
        const float* qp = (const float*)(lds + L_QP) + (lane & 31);
        const float q = (qp[0] + qp[32]) + (qp[64] + qp[96]);
        const u32x2 gr = {tag_lo | (pk_bf16(q, 0.f) & 0xffffu), tag_hi};
        __builtin_amdgcn_raw_buffer_store_b64(gr, rW, lane < 32 ? WS_QG + (g * 512 + s * 32 + lane) * 8 : OOB, 0, AUX_SC1);
      }
    }
    MEMFENCE();
    __builtin_amdgcn_s_setprio(0);
  }

  float m = NEG, l = 0.f, o[8];
  {
    const auto rK = __builtin_amdgcn_make_buffer_rsrc((void*)p.k, 0, 0x10000000, 0x00020000);
    const auto rV = __builtin_amdgcn_make_buffer_rsrc((void*)p.v, 0, 0x10000000, 0x00020000);
    u32x4 kb[2][8], vb[2][8];
    const u32 koff = (4 * (lc & 3) + (lc >> 2)) * 512 + lg * 16;
    const u32 voff = lg * 512 + lc * 16;
    const u32 tbase = g * (u32)(SK * 512) + (pdep<TMASK>(s) << 13);
    auto toff = [&](u32 i) -> u32 { return tbase + (pdep<~TMASK & 0xfff>(i) << 13); };
    auto loadK = [&](int d, u32 so) {
      u32 oo = koff; asm volatile("" : "+v"(oo));
#pragma unroll
      for (int j = 0; j < 8; ++j) kb[d][j] = __builtin_amdgcn_raw_buffer_load_b128(rK, oo + j * 64, so, AUX_NT);
      MEMFENCE(); SCHED();
    };
    auto loadV = [&](int d, u32 so) {
      u32 oo = voff; asm volatile("" : "+v"(oo));
#pragma unroll
      for (int j = 0; j < 8; ++j) vb[d][j] = __builtin_amdgcn_raw_buffer_load_b128(rV, oo + ((j >> 1) & 1) * 2048 + (j & 1) * 256, so + (j >> 2) * 4096, AUX_NT);
      MEMFENCE(); SCHED();
    };
    const u32 pair0 = w ^ 4;
    {
      const u32 so0 = toff(2 * pair0), so1 = toff(2 * pair0 + 1);
      asm volatile("" :: "s"(so0), "s"(so1), "v"(koff), "v"(voff));
      if (pair0) lds_wait(C_TURN, pair0);
      loadK(0, so0);
      if (w == 4) { __builtin_amdgcn_s_setprio(0); bar(); }
      loadV(0, so0); loadK(1, so1); loadV(1, so1);
      if (lane == 0) __hip_atomic_store(cnt + C_TURN, pair0 + 1, __ATOMIC_RELAXED, __HIP_MEMORY_SCOPE_WORKGROUP);
      MEMFENCE();
    }
    if (w == 5) {
      take_epoch();
      {
        const u32 qgoff = WS_QG + (g * 512 + lane * 2) * 8;
        u32x4 q[4];
        bool ok[4] = {false, false, false, false};
        wait_until(T_QPOLL);
        for (u32 spins = 0; spins < SPIN_CAP; ++spins) {
          MEMFENCE();
#pragma unroll
          for (int i = 0; i < 4; ++i) if (!ok[i]) q[i] = __builtin_amdgcn_raw_buffer_load_b128(rW, qgoff + i * 1024, 0, AUX_SC1);
#pragma unroll
          for (int i = 0; i < 4; ++i) ok[i] = ((q[i][0] & 0xffff0000u) == tag_lo) & (q[i][1] == tag_hi) & ((q[i][2] & 0xffff0000u) == tag_lo) & (q[i][3] == tag_hi);
          if (__all(ok[0] & ok[1] & ok[2] & ok[3])) break;
        }
#pragma unroll
        for (int i = 0; i < 4; ++i) *(u32*)(lds + L_Q + (i >> 1) * QSTR + (i & 1) * 256 + lane * 4) = (q[i][0] & 0xffffu) | (q[i][2] << 16);
      }
      if (lane == 0) *(u64*)(lds + L_EP) = epn;
      lds_arrive(C_QRDY);
    }
    lds_wait(C_QRDY, 1);
    if (w == 4) lds_wait(C_TURN, 8);
    bf16x8 qf[8];
#pragma unroll
    for (int j = 0; j < 8; ++j) qf[j] = *(const bf16x8*)(lds + L_Q + (lc & 1) * QSTR + lg * 16 + j * 64);
    constexpr float C2 = 0.0625f * 1.4426950408889634f;
    f32x4 acc[16];
#pragma unroll
    for (int i = 0; i < 16; ++i) acc[i] = f32x4{0.f, 0.f, 0.f, 0.f};
    auto qk = [&](int d) -> f32x4 {
      f32x4 sc = {0.f, 0.f, 0.f, 0.f};
#pragma unroll
      for (int j = 0; j < 8; ++j) sc = __builtin_amdgcn_mfma_f32_16x16x32_bf16(__builtin_bit_cast(bf16x8, kb[d][j]), qf[j], sc, 0, 0, 0);
      asm volatile("" : "+v"(sc) :: "memory"); SCHED();
      return sc;
    };
    auto pv = [&](int d, f32x4 sc) {
      const u32 sa = pk_bf16(sc[0], sc[1]), sb = pk_bf16(sc[2], sc[3]);
      const float t0 = bf2f_lo(sa), t1 = bf2f_hi(sa), t2 = bf2f_lo(sb), t3 = bf2f_hi(sb);
      const float tl = fmaxf(fmaxf(t0, t1), fmaxf(t2, t3)) * C2;
      if (__any(tl > m)) {
        const float mn = fmaxf(m, x32_max(x16_max(tl))), al = ex2(m - mn);
        m = mn; l *= al;
        const float a0 = dpp<0xA0>(al), a1 = dpp<0xF5>(al);
#pragma unroll
        for (int i = 0; i < 16; ++i) { acc[i][0] *= a0; acc[i][1] *= a1; }
      }
      const float p0 = ex2(fmaf(t0, C2, -m)), p1 = ex2(fmaf(t1, C2, -m)), p2 = ex2(fmaf(t2, C2, -m)), p3 = ex2(fmaf(t3, C2, -m));
      l += (p0 + p1) + (p2 + p3);
      const u32x2 pp = {pk_bf16(p0, p1), pk_bf16(p2, p3)};
      const s16x4 pf = __builtin_bit_cast(s16x4, pp);
#pragma unroll
      for (int hf = 0; hf < 2; ++hf)
#pragma unroll
        for (int e = 0; e < 8; ++e) {
          const u32 sel = (e & 1) ? 0x07060302u : 0x05040100u;
          const u32x2 bb = {__builtin_amdgcn_perm(vb[d][2 + hf][e >> 1], vb[d][hf][e >> 1], sel), __builtin_amdgcn_perm(vb[d][6 + hf][e >> 1], vb[d][4 + hf][e >> 1], sel)};
          acc[hf * 8 + e] = __builtin_amdgcn_mfma_f32_16x16x16bf16_1k(pf, __builtin_bit_cast(s16x4, bb), acc[hf * 8 + e], 0, 0, 0);
        }
      MEMFENCE(); SCHED();
    };
    if (w == 4) {
      for (u32 tp = 8; tp < NPAIR; ++tp) {
#pragma unroll
        for (int d = 0; d < 2; ++d) {
          const u32 so = toff(2 * tp + d);
          const f32x4 sc = qk(d);
          loadK(d, so); pv(d, sc); loadV(d, so);
        }
      }
    }
#pragma unroll
    for (int d = 0; d < 2; ++d) { const f32x4 sc = qk(d); pv(d, sc); }
    l = x32_sum(x16_sum(l));
#pragma unroll
    for (int e = 0; e < 8; ++e) { const float lo = (lg & 1) ? acc[e][1] : acc[e][0], hi = (lg & 1) ? acc[8 + e][1] : acc[8 + e][0]; o[e] = (lg >> 1) ? hi : lo; }
  }
  const int ridx = (lg & 1) * 256 + (lg >> 1) * 128 + lc * 8;
  if (w != 4) {
    float* red = (float*)(lds + L_RED) + ridx;
    float* ml = (float*)(lds + L_ML);
    *(f32x4*)(red + w * 512) = f32x4{o[0], o[1], o[2], o[3]}; *(f32x4*)(red + w * 512 + 4) = f32x4{o[4], o[5], o[6], o[7]};
    if (lane < 2) *(float2*)(ml + w * 4 + lane * 2) = make_float2(m, l);
    if (lds_arrive(C_PART) == 6) {
      const int h2 = (lg & 1) * 2;
      float M = NEG, L = 0.f;
      f32x4 a0 = {0.f, 0.f, 0.f, 0.f}, a1 = a0;
#pragma unroll
      for (int i = 0; i < 8; ++i) if (i != 4) M = fmaxf(M, ml[i * 4 + h2]);
#pragma unroll
      for (int i = 0; i < 8; ++i) if (i != 4) { const float e = ex2(ml[i * 4 + h2] - M);
        L = fmaf(ml[i * 4 + h2 + 1], e, L); a0 += *(const f32x4*)(red + i * 512) * e; a1 += *(const f32x4*)(red + i * 512 + 4) * e; }
      *(f32x4*)(red + 4 * 512) = a0; *(f32x4*)(red + 4 * 512 + 4) = a1;
      if (lc == 0 && lg < 2) *(float2*)(ml + 16 + h2) = make_float2(M, L);
      lds_arrive(C_MERGED);
    }
  } else {
    if (lane == 0) __hip_atomic_store(cnt + C_END, 1u, __ATOMIC_RELAXED, __HIP_MEMORY_SCOPE_WORKGROUP);
    lds_wait(C_MERGED, 1);
    const float* red = (const float*)(lds + L_RED) + 4 * 512 + ridx;
    const f32x4 ma = *(const f32x4*)((const float*)(lds + L_ML) + 16);
    const f32x4 a0 = *(const f32x4*)red, a1 = *(const f32x4*)(red + 4);
    const float m0 = dpp<0xA0>(m), m1 = dpp<0xF5>(m), l0 = dpp<0xA0>(l), l1 = dpp<0xF5>(l);
    const float mh = (lg & 1) ? m1 : m0, lh = (lg & 1) ? l1 : l0, Ma = (lg & 1) ? ma[2] : ma[0], La = (lg & 1) ? ma[3] : ma[1];
    const float M = fmaxf(mh, Ma), e0 = ex2(mh - M), e1 = ex2(Ma - M);
    const u32 rec = WS_RA + (g * 32 + s) * RA_BLK + ((lg & 1) * 16 + (lg >> 1) * 8 + (lc >> 1)) * 80;
    const u32x2 ep2 = *(const u32x2*)(lds + L_EP);
    u32x4 v0, v1;
#pragma unroll
    for (int e = 0; e < 4; ++e) { v0[e] = __float_as_uint(fmaf(o[e], e0, a0[e] * e1)); v1[e] = __float_as_uint(fmaf(o[4 + e], e0, a1[e] * e1)); }
    const u32 Mb = __float_as_uint(M), Lb = __float_as_uint(fmaf(lh, e0, La * e1));
    {
    __builtin_amdgcn_raw_buffer_store_b128(v0, rW, rec + (lc & 1) * 32, 0, AUX_SC1);
    __builtin_amdgcn_raw_buffer_store_b128(v1, rW, rec + (lc & 1) * 32 + 16, 0, AUX_SC1);
    u32 ca = 0, cb = 0;
#pragma unroll
    for (int e = 0; e < 4; ++e) { chk(ca, cb, v0[e], (lc & 1) * 8 + e); chk(ca, cb, v1[e], (lc & 1) * 8 + 4 + e); }
    ca += dppu<XOR1>(ca); cb ^= dppu<XOR1>(cb);
    ca += ep2[0]; cb ^= ep2[1];
    chk(ca, cb, Mb, 16); chk(ca, cb, Lb, 17);
    __builtin_amdgcn_raw_buffer_store_b128(u32x4{Mb, Lb, ca, cb}, rW, (lc & 1) ? OOB : rec + 64, 0, AUX_SC1);
    }
    MEMFENCE();
  }

  auto publish_c = [&](float y0, float y1) {
    const u32 b0 = __float_as_uint(y0), b1 = __float_as_uint(y1);
    u32 ca = 0, cb = 0;
    chk(ca, cb, b0, (lane & 7) * 2); chk(ca, cb, b1, (lane & 7) * 2 + 1);
    ca += dppu<XOR1>(ca); cb ^= dppu<XOR1>(cb); ca += dppu<XOR2>(ca); cb ^= dppu<XOR2>(cb); ca += dppu<XOR7>(ca); cb ^= dppu<XOR7>(cb);
    ca += (u32)epn; cb ^= (u32)(epn >> 32);
    const u32 z0 = dppu<XOR1>(b0), z1 = dppu<XOR1>(b1);
    const u32 rec = WS_RC + (w * 7 + (lane >> 3)) * RC_BLK + cslot * 128;
    const bool odd = lane & 1;
    const u32x4 ov = {odd ? ca : b0, odd ? cb : b1, odd ? ca : z0, odd ? cb : z1};
    __builtin_amdgcn_raw_buffer_store_b128(ov, rW, lane >= 56 ? OOB : !odd ? rec + (lane & 7) * 8 : (lane & 7) == 1 ? rec + 64 : OOB, 0, AUX_SC1);
    MEMFENCE();
  };
  auto gather_c = [&](int slot0, int nslot, u32 e0, u32 e1) {
    const u32 off = WS_RC + fid * RC_BLK + (slot0 + (lane >> 2)) * 128;
    u32x4 yv = {0u, 0u, 0u, 0u}; u32x2 cv;
    bool ok = (lane >> 2) >= nslot;
    for (u32 spins = 0; spins < SPIN_CAP; ++spins) {
      MEMFENCE();
      if (!ok) {
        yv = __builtin_amdgcn_raw_buffer_load_b128(rW, off + (lane & 3) * 16, 0, AUX_SC1);
        cv = __builtin_amdgcn_raw_buffer_load_b64(rW, off + 64, 0, AUX_SC1);
        u32 ca = 0, cb = 0;
#pragma unroll
        for (int e = 0; e < 4; ++e) chk(ca, cb, yv[e], (lane & 3) * 4 + e);
        ca += dppu<XOR1>(ca); cb ^= dppu<XOR1>(cb); ca += dppu<XOR2>(ca); cb ^= dppu<XOR2>(cb);
        u32 bad = ((ca + e0) ^ cv[0]) | ((cb ^ e1) ^ cv[1]);
        bad |= dppu<XOR1>(bad); bad |= dppu<XOR2>(bad);
        ok = bad == 0;
      }
      const int miss = __popcll(__ballot(!ok));
      if (miss == 0) break;
      for (int i = 8; i < miss; i += 8) __builtin_amdgcn_s_sleep(6);
    }
    const float v0 = t32_sum(__uint_as_float(yv[0]), __uint_as_float(yv[2])), v1 = t32_sum(__uint_as_float(yv[1]), __uint_as_float(yv[3]));
    float t = t16_sum(v0, v1);
    t += dpp<ROR4>(t); t += dpp<XOR8>(t);
    if (!(lane & 12)) ((float*)(lds + L_FIN))[(w ^ 4) * 16 + (lane & 3) * 4 + lg] = t;
  };

  u32 wp[64];
  if (w < 4 && (has_mlp || cons)) {
    const u32 rowoff = lane < 56 ? lane * 16 : OOB;
    const int l2 = w * 56 + (lane < 56 ? lane : 0);
    wait_until(T_CHORE + (((s - 16) & ~1) * 4 + (cons ? (s & 1) * 4 : 0) + w) * T_STAG);
    if (has_mlp) {
      const auto rW1 = __builtin_amdgcn_make_buffer_rsrc((void*)p.w1, 0, D * FF * 2, 0x00020000);
      const auto rW2 = __builtin_amdgcn_make_buffer_rsrc((void*)p.w2, 0, D * FF * 2, 0x00020000);
      const auto rB1 = __builtin_amdgcn_make_buffer_rsrc((void*)p.b1, 0, FF * 2, 0x00020000);
      const int u0 = mi * 32;
      u32x4 w1r[7], w2r[8];
#pragma unroll
      for (int i = 0; i < 7; ++i) w1r[i] = __builtin_amdgcn_raw_buffer_load_b128(rW1, (w * 16 + (lane >> 2)) * (FF * 2) + u0 * 2 + (lane & 3) * 16, i * 64 * (FF * 2), AUX_NT);
#pragma unroll
      for (int i = 0; i < 8; ++i) w2r[i] = __builtin_amdgcn_raw_buffer_load_b128(rW2, rowoff, (u0 + w * 8 + i) * 896, AUX_NT);
      const u32 b1r = __builtin_amdgcn_raw_buffer_load_b16(rB1, (u0 + w * 8 + (lane & 7)) * 2, 0, 0);
      gemv32(w1r);
      lds_arrive(C_HP);
      lds_wait(C_HP, 4);
      const float* hp = (const float*)(lds + L_QP) + w * 8 + (lane & 7);
      float hv = bfr(bfr((hp[0] + hp[32]) + (hp[64] + hp[96])) + bf2f_lo(b1r));
      hv = bfr(0.5f * hv * (1.f + tanhf(0.7978845608028654f * (hv + 0.044715f * hv * hv * hv))));
      float ym[8];
#pragma unroll
      for (int e = 0; e < 8; ++e) ym[e] = 0.f;
#pragma unroll
      for (int r = 0; r < 8; ++r) {
        const float hr = __builtin_bit_cast(float, __builtin_amdgcn_readlane(__builtin_bit_cast(int, hv), r));
#pragma unroll
        for (int e = 0; e < 4; ++e) { ym[2 * e] = fmaf(hr, bf2f_lo(w2r[r][e]), ym[2 * e]); ym[2 * e + 1] = fmaf(hr, bf2f_hi(w2r[r][e]), ym[2 * e + 1]); }
      }
      if (lane < 56) { float* y = (float*)(lds + L_YM) + w * 448 + lane * 8;
        *(f32x4*)y = f32x4{ym[0], ym[1], ym[2], ym[3]}; *(f32x4*)(y + 4) = f32x4{ym[4], ym[5], ym[6], ym[7]}; }
      lds_arrive(C_CHORE);
      lds_wait(C_CHORE, 4);
      const float2* y = (const float2*)(lds + L_YM) + l2;
      const float2 a = y[0], b = y[224], c = y[448], d = y[672];
      lds_wait(C_END, 1);
      publish_c((a.x + b.x) + (c.x + d.x), (a.y + b.y) + (c.y + d.y));
    } else {
      const auto rWo = __builtin_amdgcn_make_buffer_rsrc((void*)p.wo, 0, D * HQ * DH * 2, 0x00020000);
      u32x4 wor[16];
#pragma unroll
      for (int i = 0; i < 16; ++i) wor[i] = __builtin_amdgcn_raw_buffer_load_b128(rWo, rowoff, (g * 512 + (s - 24) * 64 + w * 16 + i) * 896, AUX_NT);
      if (lane < 56) {
#pragma unroll
        for (int i = 0; i < 16; ++i) *(u32x4*)(lds + L_WO + (w * 16 + i) * 896 + lane * 16) = wor[i];
      }
      lds_arrive(C_CHORE);
      if (fin) {
        if (tid < 16) { const int col = fid * 16 + tid;
          ((float*)(lds + L_XB))[tid] = __bfloat162float(((const __hip_bfloat16*)p.x)[col]) + __bfloat162float(((const __hip_bfloat16*)p.b2)[col]); }
      }
      lds_wait(C_CHORE, 4);
#pragma unroll
      for (int k = 0; k < 32; ++k) {
        const u32 a = ((const u32*)(lds + L_WO))[(2 * k) * 224 + l2], b = ((const u32*)(lds + L_WO))[(2 * k + 1) * 224 + l2];
        wp[2 * k] = __builtin_amdgcn_perm(b, a, 0x05040100u); wp[2 * k + 1] = __builtin_amdgcn_perm(b, a, 0x07060302u);
      }
    }
  }
  if (cons) {
    bar();
    const u32x2 ep2 = *(const u32x2*)(lds + L_EP);
    if (w >= 4) {
      const u32 roff = WS_RA + (g * 32 + (lane >> 2)) * RA_BLK + ((s - 24) * 4 + w - 4) * 80;
      u32x4 ov[2], mv[2];
      bool ok[2] = {false, false};
      for (u32 spins = 0; spins < SPIN_CAP; ++spins) {
        MEMFENCE();
#pragma unroll
        for (int i = 0; i < 2; ++i) if (!ok[i]) {
          ov[i] = __builtin_amdgcn_raw_buffer_load_b128(rW, roff + i * 16 * RA_BLK + (lane & 3) * 16, 0, AUX_SC1);
          mv[i] = __builtin_amdgcn_raw_buffer_load_b128(rW, roff + i * 16 * RA_BLK + 64, 0, AUX_SC1);
        }
        int miss = 0;
#pragma unroll
        for (int i = 0; i < 2; ++i) {
          u32 ca = 0, cb = 0;
#pragma unroll
          for (int e = 0; e < 4; ++e) chk(ca, cb, ov[i][e], (lane & 3) * 4 + e);
          ca += dppu<XOR1>(ca); cb ^= dppu<XOR1>(cb); ca += dppu<XOR2>(ca); cb ^= dppu<XOR2>(cb);
          ca += ep2[0]; cb ^= ep2[1];
          chk(ca, cb, mv[i][0], 16); chk(ca, cb, mv[i][1], 17);
          u32 bad = (ca ^ mv[i][2]) | (cb ^ mv[i][3]);
          bad |= dppu<XOR1>(bad); bad |= dppu<XOR2>(bad);
          ok[i] = bad == 0;
          miss += __popcll(__ballot(!ok[i]));
        }
        if (miss == 0) break;
        for (int i = 8; i < miss; i += 4) __builtin_amdgcn_s_sleep(2);
      }
      const float m0 = __uint_as_float(mv[0][0]), m1 = __uint_as_float(mv[1][0]);
      float M = fmaxf(m0, m1);
      M = fmaxf(M, dpp<ROR4>(M)); M = fmaxf(M, dpp<XOR8>(M)); M = x32_max(x16_max(M));
      const float w0 = ex2(m0 - M), w1 = ex2(m1 - M);
      float L = fmaf(w0, __uint_as_float(mv[0][1]), w1 * __uint_as_float(mv[1][1]));
      L += dpp<ROR4>(L); L += dpp<XOR8>(L); L = x32_sum(x16_sum(L));
      float oo[4];
#pragma unroll
      for (int e = 0; e < 4; ++e) oo[e] = fmaf(w0, __uint_as_float(ov[0][e]), w1 * __uint_as_float(ov[1][e]));
      oo[0] = t32_sum(oo[0], oo[2]); oo[1] = t32_sum(oo[1], oo[3]);
      float t = t16_sum(oo[0], oo[1]);
      t += dpp<ROR4>(t); t += dpp<XOR8>(t);
      if (!(lane & 12)) ((unsigned short*)(lds + L_O))[(w - 4) * 16 + (lane & 3) * 4 + lg] = (unsigned short)pk_bf16(t * __builtin_amdgcn_rcpf(L), 0.f);
    }
    bar();
    if (w < 4) {
      float y0 = 0.f, y1 = 0.f, y2 = 0.f, y3 = 0.f;
#pragma unroll
      for (int k4 = 0; k4 < 8; ++k4) { const u32x4 op = ((const u32x4*)(lds + L_O))[k4];
#pragma unroll
        for (int e = 0; e < 4; e += 2) {
          y0 = dot2(wp[8 * k4 + 2 * e], op[e], y0); y1 = dot2(wp[8 * k4 + 2 * e + 1], op[e], y1);
          y2 = dot2(wp[8 * k4 + 2 * e + 2], op[e + 1], y2); y3 = dot2(wp[8 * k4 + 2 * e + 3], op[e + 1], y3);
        } }
      publish_c(y0 + y2, y1 + y3);
    }
    if (fin) {
      gather_c((w ^ 4) * 16, w == 3 ? 8 : 16, ep2[0], ep2[1]);
      bar();
      if (tid < 16) {
        const float* f = (const float*)(lds + L_FIN) + tid;
        const float r = (((const float*)(lds + L_XB))[tid] + ((f[64] + f[80]) + (f[96] + f[112]))) + ((f[0] + f[16]) + (f[32] + f[48]));
        ((unsigned short*)p.out)[fid * 16 + tid] = (unsigned short)pk_bf16(r, 0.f);
        if (fid == 0 && tid == 0) __builtin_amdgcn_raw_buffer_store_b64(ep2, rW, WS_EPOCH, 0, AUX_SC1);
      }
    }
  }
}

extern "C" void kernel_launch(void* const* d_in, const int* in_sizes, int n_in, void* d_out, int out_size, void* d_ws, size_t ws_size, hipStream_t stream) {
  Params p;
  p.x = d_in[0]; p.k = d_in[1]; p.v = d_in[2]; p.wq = d_in[3]; p.wo = d_in[4];
  p.w1 = d_in[6]; p.b1 = d_in[7]; p.w2 = d_in[8]; p.b2 = d_in[9];
  p.out = d_out; p.ws = d_ws;
  hipLaunchKernelGGL(fused_decode, dim3(NWG), dim3(512), 0, stream, p);
}
```

```cpp
#include <hip/hip_runtime.h>
#include <hip/hip_bf16.h>
#include <stdint.h>

typedef unsigned u32;
typedef unsigned long long u64;
typedef u32 u32x4 __attribute__((ext_vector_type(4)));
typedef u32 u32x2 __attribute__((ext_vector_type(2)));
typedef float f32x4 __attribute__((ext_vector_type(4)));
typedef __bf16 bf16x8 __attribute__((ext_vector_type(8)));
typedef __bf16 bf16x2 __attribute__((ext_vector_type(2)));
typedef short s16x4 __attribute__((ext_vector_type(4)));
#define DI __device__ __forceinline__

constexpr u32 TMASK = 0x178;
constexpr u32 T_QPOLL = 450, T_CHORE = 1200, T_STAG = 20;

constexpr int D = 448, HQ = 16, SK = 65536, DH = 256, FF = 1792;
constexpr int NWG = 256;
constexpr int NPAIR = 64;
constexpr int NFIN = 28;
constexpr u32 WS_EPOCH = 0;
constexpr u32 WS_QG = 4096;
constexpr u32 WS_RA = WS_QG + 8 * 512 * 8;
constexpr u32 RA_BLK = 32 * 80;
constexpr u32 WS_RC = WS_RA + NWG * RA_BLK;
constexpr u32 RC_BLK = 128 * 128;
constexpr u32 WS_END = WS_RC + NFIN * RC_BLK;
constexpr int AUX_NT = 2, AUX_SC1 = 16;
constexpr float NEG = -3.0e38f;
constexpr u32 SPIN_CAP = 1u << 15;
constexpr u32 OOB = 0x7fffff00u;

#define MEMFENCE() asm volatile("" ::: "memory")
#define SCHED() __builtin_amdgcn_sched_barrier(0)

DI float bf2f_lo(u32 x) { return __builtin_bit_cast(float, x << 16); }
DI float bf2f_hi(u32 x) { return __builtin_bit_cast(float, x & 0xffff0000u); }
typedef float f32x2 __attribute__((ext_vector_type(2)));
DI u32 pk_bf16(float a, float b) { const f32x2 f = {a, b}; return __builtin_bit_cast(u32, __builtin_convertvector(f, bf16x2)); }
DI float bfr(float a) { return bf2f_lo(pk_bf16(a, 0.f)); }
template <int CTRL> DI float dpp(float x) {
  return __builtin_bit_cast(float, __builtin_amdgcn_mov_dpp(__builtin_bit_cast(int, x), CTRL, 0xf, 0xf, true));
}
constexpr int XOR1 = 0xB1, XOR2 = 0x4E, XOR3 = 0x1B, XOR7 = 0x141, XOR8 = 0x128, ROR4 = 0x124;
DI float t32_sum(float u, float v) { auto s = __builtin_amdgcn_permlane32_swap(__float_as_uint(u), __float_as_uint(v), false, false); return __uint_as_float(s[0]) + __uint_as_float(s[1]); }
DI float t16_sum(float u, float v) { auto s = __builtin_amdgcn_permlane16_swap(__float_as_uint(u), __float_as_uint(v), false, false); return __uint_as_float(s[0]) + __uint_as_float(s[1]); }
DI float x16_sum(float x) { return t16_sum(x, x); }
DI float x32_sum(float x) { return t32_sum(x, x); }
DI float x16_max(float x) { auto s = __builtin_amdgcn_permlane16_swap(__float_as_uint(x), __float_as_uint(x), false, false); return fmaxf(__uint_as_float(s[0]), __uint_as_float(s[1])); }
DI float x32_max(float x) { auto s = __builtin_amdgcn_permlane32_swap(__float_as_uint(x), __float_as_uint(x), false, false); return fmaxf(__uint_as_float(s[0]), __uint_as_float(s[1])); }
DI float row_sum(float x) { x += dpp<XOR1>(x); x += dpp<XOR2>(x); x += dpp<XOR7>(x); x += dpp<XOR8>(x); return x; }
DI float wave_sum(float x) { return x32_sum(x16_sum(row_sum(x))); }
template <int CTRL> DI u32 dppu(u32 x) { return (u32)__builtin_amdgcn_mov_dpp((int)x, CTRL, 0xf, 0xf, true); }
DI u32 rotl(u32 x, u32 r) { return __builtin_amdgcn_alignbit(x, x, 32u - r); }
DI void chk(u32& ca, u32& cb, u32 v, u32 i) { ca += v; cb ^= rotl(v, i + 1); }
DI void bar() { asm volatile("s_waitcnt lgkmcnt(0)\n\ts_barrier" ::: "memory"); }
DI float dot2(u32 a, u32 b, float c) { return __builtin_amdgcn_fdot2_f32_bf16(__builtin_bit_cast(bf16x2, a), __builtin_bit_cast(bf16x2, b), c, false); }
DI float ex2(float x) { return __builtin_amdgcn_exp2f(x); }
template <u32 M> DI u32 pdep(u32 x) {
  u32 r = 0, k = 0;
#pragma unroll
  for (int b = 0; b < 12; ++b) if ((M >> b) & 1) { r |= ((x >> k) & 1u) << b; ++k; }
  return r;
}

struct Params {
  const void* x; const void* k; const void* v; const void* wq; const void* wo;
  const void* w1; const void* b1; const void* w2; const void* b2;
  void* out; void* ws;
};

constexpr int L_XN = 0;
constexpr int QSTR = 576;
constexpr int L_Q = L_XN + 2048;
constexpr int L_WO = L_Q + 2 * QSTR;
constexpr int L_QP = L_WO + 64 * 896;
constexpr int L_YM = L_QP + 512;
constexpr int L_RED = L_YM + 4 * 448 * 4;
constexpr int L_ML = L_RED + 8 * 2048;
constexpr int L_O = L_ML + 128;
constexpr int L_FIN = L_O + 128;
constexpr int L_XB = L_FIN + 512;
constexpr int L_EP = L_XB + 64;
constexpr int L_CNT = L_EP + 16;
constexpr int L_END = L_CNT + 64;
enum { C_QPART, C_QRDY, C_HP, C_CHORE, C_TURN, C_PART, C_MERGED, C_END };

__global__ __launch_bounds__(512, 2) void fused_decode(Params p) {
  __shared__ __attribute__((aligned(16))) char lds[L_END];
  const int tid = threadIdx.x, lane = tid & 63;
  const int w = __builtin_amdgcn_readfirstlane(tid >> 6);
  const int bid = blockIdx.x;
  const int g = ((bid >> 6) << 1) | ((bid >> 2) & 1);
  const int s = (((bid >> 4) & 3) << 3) | ((bid & 3) << 1) | ((bid >> 3) & 1);
  const int lg = lane >> 4, lc = lane & 15;
  if (w == 4) __builtin_amdgcn_s_setprio(3);
  const auto rW = __builtin_amdgcn_make_buffer_rsrc(p.ws, 0, WS_END, 0x00020000);
  u64 epn = 0;
  u32* const cnt = (u32*)(lds + L_CNT);
  float* const xn = (float*)(lds + L_XN);
  auto lds_arrive = [&](int i) -> u32 {
    asm volatile("s_waitcnt lgkmcnt(0)" ::: "memory");
    u32 r = 0;
    if (lane == 0) r = __hip_atomic_fetch_add(cnt + i, 1u, __ATOMIC_RELAXED, __HIP_MEMORY_SCOPE_WORKGROUP);
    r = __builtin_amdgcn_readfirstlane(r);
    MEMFENCE();
    return r;
  };
  auto lds_wait = [&](int i, u32 v) { while (__hip_atomic_load(cnt + i, __ATOMIC_RELAXED, __HIP_MEMORY_SCOPE_WORKGROUP) != v) __builtin_amdgcn_s_sleep(1); MEMFENCE(); };
  const u64 tstart = __builtin_amdgcn_s_memrealtime();
  u32 dmark = 0;
  auto wait_until = [&](u32 ticks) { while ((u32)(__builtin_amdgcn_s_memrealtime() - tstart) < ticks) __builtin_amdgcn_s_sleep(1); };
  const bool q_prod = s < 16, has_mlp = s >= 16 && s < (g < 4 ? 24 : 22), cons = s >= 24;
  const int mi = (g < 4 ? g * 8 : 8 + g * 6) + s - 16;
  const int cslot = cons ? (s - 24) * 8 + g : 64 + mi;
  const int fid = (s - 24) * 8 + g;
  const bool fin = cons && fid < NFIN;
  auto gemv32 = [&](const u32x4 (&wr)[7]) {
    float a[8];
#pragma unroll
    for (int e = 0; e < 8; ++e) a[e] = 0.f;
#pragma unroll
    for (int i = 0; i < 7; ++i) { const float xx = xn[16 * (w + 4 * i) + (lane >> 2)];
#pragma unroll
      for (int e = 0; e < 4; ++e) { a[2 * e] = fmaf(xx, bf2f_lo(wr[i][e]), a[2 * e]); a[2 * e + 1] = fmaf(xx, bf2f_hi(wr[i][e]), a[2 * e + 1]); } }
#pragma unroll
    for (int e = 0; e < 4; ++e) a[e] = t32_sum(a[e], a[e + 4]);
#pragma unroll
    for (int e = 0; e < 2; ++e) a[e] = t16_sum(a[e], a[e + 2]);
    const float t1 = a[0] + dpp<XOR8>(a[0]), t2 = a[1] + dpp<XOR8>(a[1]);
    float t = (lane & 8) ? t2 : t1;
    t += dpp<XOR7>(dpp<XOR3>(t));
    if (!(lane & 4)) ((float*)(lds + L_QP))[w * 32 + (lane & 3) * 8 + (lane >> 3)] = t;
  };
  u32x4 xr, wq[7];
  u32x2 ep = {0u, 0u};
  if (w < 4 || w == 5) ep = __builtin_amdgcn_raw_buffer_load_b64(rW, WS_EPOCH, 0, AUX_SC1);
  u32 tag_lo = 0, tag_hi = 0;
  auto take_epoch = [&]() {
    asm volatile("" : "+v"(ep));
    const u64 ep64 = ((u64)(u32)__builtin_amdgcn_readfirstlane(ep[1]) << 32) | (u32)__builtin_amdgcn_readfirstlane(ep[0]);
    epn = ep64 * 6364136223846793005ull + 1442695040888963407ull;
    tag_lo = (u32)epn << 16; tag_hi = (u32)(epn >> 16);
  };
  if (w < 4) {
    __builtin_amdgcn_s_setprio(2);
    const auto rX = __builtin_amdgcn_make_buffer_rsrc((void*)p.x, 0, (q_prod || has_mlp) ? D * 2 : 0, 0x00020000);
    const auto rWq = __builtin_amdgcn_make_buffer_rsrc((void*)p.wq, 0, q_prod ? D * HQ * DH * 2 : 0, 0x00020000);
    xr = __builtin_amdgcn_raw_buffer_load_b128(rX, lane * 16, 0, 0);
    const u32 wqo = (w * 16 + (lane >> 2)) * 8192 + (g * 512 + s * 32) * 2 + (lane & 3) * 16;
#pragma unroll
    for (int i = 0; i < 7; ++i) wq[i] = __builtin_amdgcn_raw_buffer_load_b128(rWq, wqo, i * 64 * 8192, AUX_NT);
  }
  if (w != 4) {
    if (tid < 16) cnt[tid] = 0;
    bar();
  }
  if (__builtin_expect(w < 4, 0)) {
    {
      float xv[8];
#pragma unroll
      for (int e = 0; e < 4; ++e) { xv[2 * e] = bf2f_lo(xr[e]); xv[2 * e + 1] = bf2f_hi(xr[e]); }
      const float mean = wave_sum(((xv[0] + xv[1]) + (xv[2] + xv[3])) + ((xv[4] + xv[5]) + (xv[6] + xv[7]))) * (1.f / D);
      float sq = 0.f;
#pragma unroll
      for (int e = 0; e < 8; ++e) { const float d = xv[e] - mean; sq = fmaf(d, d, sq); }
      sq = lane < 56 ? sq : 0.f;
      const float var = wave_sum(sq) * (1.f / D);
      const float mb = bfr(mean), den = bfr(__builtin_amdgcn_sqrtf(bfr(bfr(var) + 1e-5f)));
      const float rden = lane < 56 ? __builtin_amdgcn_rcpf(den) : 0.f;
      f32x4 o[2];
#pragma unroll
      for (int e = 0; e < 8; ++e) o[e >> 2][e & 3] = bfr(bfr(xv[e] - mb) * rden);
      *(f32x4*)(xn + lane * 8) = o[0]; *(f32x4*)(xn + lane * 8 + 4) = o[1];
    }
    take_epoch();
    if (q_prod) {
      gemv32(wq);
      if (lds_arrive(C_QPART) == 3) {
        const float* qp = (const float*)(lds + L_QP) + (lane & 31);
        const float q = (qp[0] + qp[32]) + (qp[64] + qp[96]);
        const u32x2 gr = {tag_lo | (pk_bf16(q, 0.f) & 0xffffu), tag_hi};
        __builtin_amdgcn_raw_buffer_store_b64(gr, rW, lane < 32 ? WS_QG + (g * 512 + s * 32 + lane) * 8 : OOB, 0, AUX_SC1);
      }
    }
    MEMFENCE();
    __builtin_amdgcn_s_setprio(0);
  }

  float m = NEG, l = 0.f, o[8];
  {
    const auto rK = __builtin_amdgcn_make_buffer_rsrc((void*)p.k, 0, 0x10000000, 0x00020000);
    const auto rV = __builtin_amdgcn_make_buffer_rsrc((void*)p.v, 0, 0x10000000, 0x00020000);
    u32x4 kb[2][8], vb[2][8];
    const u32 koff = (4 * (lc & 3) + (lc >> 2)) * 512 + lg * 16;
    const u32 voff = lg * 512 + lc * 16;
    const u32 tbase = g * (u32)(SK * 512) + (pdep<TMASK>(s) << 13);
    auto toff = [&](u32 i) -> u32 { return tbase + (pdep<~TMASK & 0xfff>(i) << 13); };
    auto loadK = [&](int d, u32 so) {
      u32 oo = koff; asm volatile("" : "+v"(oo));
#pragma unroll
      for (int j = 0; j < 8; ++j) kb[d][j] = __builtin_amdgcn_raw_buffer_load_b128(rK, oo + j * 64, so, AUX_NT);
      MEMFENCE(); SCHED();
    };
    auto loadV = [&](int d, u32 so) {
      u32 oo = voff; asm volatile("" : "+v"(oo));
#pragma unroll
      for (int j = 0; j < 8; ++j) vb[d][j] = __builtin_amdgcn_raw_buffer_load_b128(rV, oo + ((j >> 1) & 1) * 2048 + (j & 1) * 256, so + (j >> 2) * 4096, AUX_NT);
      MEMFENCE(); SCHED();
    };
    const u32 pair0 = w ^ 4;
    {
      const u32 so0 = toff(2 * pair0), so1 = toff(2 * pair0 + 1);
      asm volatile("" :: "s"(so0), "s"(so1), "v"(koff), "v"(voff));
      if (pair0) lds_wait(C_TURN, pair0);
      loadK(0, so0);
      if (w == 4) { __builtin_amdgcn_s_setprio(0); bar(); }
      loadV(0, so0); loadK(1, so1); loadV(1, so1);
      if (lane == 0) __hip_atomic_store(cnt + C_TURN, pair0 + 1, __ATOMIC_RELAXED, __HIP_MEMORY_SCOPE_WORKGROUP);
      MEMFENCE();
    }
    if (w == 5) {
      take_epoch();
      {
        const u32 qgoff = WS_QG + (g * 512 + lane * 2) * 8;
        u32x4 q[4];
        bool ok[4] = {false, false, false, false};
        wait_until(T_QPOLL);
        for (u32 spins = 0; spins < SPIN_CAP; ++spins) {
          MEMFENCE();
#pragma unroll
          for (int i = 0; i < 4; ++i) if (!ok[i]) q[i] = __builtin_amdgcn_raw_buffer_load_b128(rW, qgoff + i * 1024, 0, AUX_SC1);
#pragma unroll
          for (int i = 0; i < 4; ++i) ok[i] = ((q[i][0] & 0xffff0000u) == tag_lo) & (q[i][1] == tag_hi) & ((q[i][2] & 0xffff0000u) == tag_lo) & (q[i][3] == tag_hi);
          if (__all(ok[0] & ok[1] & ok[2] & ok[3])) break;
        }
#pragma unroll
        for (int i = 0; i < 4; ++i) *(u32*)(lds + L_Q + (i >> 1) * QSTR + (i & 1) * 256 + lane * 4) = (q[i][0] & 0xffffu) | (q[i][2] << 16);
      }
      if (lane == 0) *(u64*)(lds + L_EP) = epn;
      lds_arrive(C_QRDY);
    }
    lds_wait(C_QRDY, 1);
    if (w == 4) lds_wait(C_TURN, 8);
    bf16x8 qf[8];
#pragma unroll
    for (int j = 0; j < 8; ++j) qf[j] = *(const bf16x8*)(lds + L_Q + (lc & 1) * QSTR + lg * 16 + j * 64);
    constexpr float C2 = 0.0625f * 1.4426950408889634f;
    f32x4 acc[16];
#pragma unroll
    for (int i = 0; i < 16; ++i) acc[i] = f32x4{0.f, 0.f, 0.f, 0.f};
    auto qk = [&](int d) -> f32x4 {
      f32x4 sc = {0.f, 0.f, 0.f, 0.f};
#pragma unroll
      for (int j = 0; j < 8; ++j) sc = __builtin_amdgcn_mfma_f32_16x16x32_bf16(__builtin_bit_cast(bf16x8, kb[d][j]), qf[j], sc, 0, 0, 0);
      asm volatile("" : "+v"(sc) :: "memory"); SCHED();
      return sc;
    };
    auto pv = [&](int d, f32x4 sc) {
      const u32 sa = pk_bf16(sc[0], sc[1]), sb = pk_bf16(sc[2], sc[3]);
      const float t0 = bf2f_lo(sa), t1 = bf2f_hi(sa), t2 = bf2f_lo(sb), t3 = bf2f_hi(sb);
      const float tl = fmaxf(fmaxf(t0, t1), fmaxf(t2, t3)) * C2;
      if (__any(tl > m)) {
        const float mn = fmaxf(m, x32_max(x16_max(tl))), al = ex2(m - mn);
        m = mn; l *= al;
        const float a0 = dpp<0xA0>(al), a1 = dpp<0xF5>(al);
#pragma unroll
        for (int i = 0; i < 16; ++i) { acc[i][0] *= a0; acc[i][1] *= a1; }
      }
      const float p0 = ex2(fmaf(t0, C2, -m)), p1 = ex2(fmaf(t1, C2, -m)), p2 = ex2(fmaf(t2, C2, -m)), p3 = ex2(fmaf(t3, C2, -m));
      l += (p0 + p1) + (p2 + p3);
      const u32x2 pp = {pk_bf16(p0, p1), pk_bf16(p2, p3)};
      const s16x4 pf = __builtin_bit_cast(s16x4, pp);
#pragma unroll
      for (int hf = 0; hf < 2; ++hf)
#pragma unroll
        for (int e = 0; e < 8; ++e) {
          const u32 sel = (e & 1) ? 0x07060302u : 0x05040100u;
          const u32x2 bb = {__builtin_amdgcn_perm(vb[d][2 + hf][e >> 1], vb[d][hf][e >> 1], sel), __builtin_amdgcn_perm(vb[d][6 + hf][e >> 1], vb[d][4 + hf][e >> 1], sel)};
          acc[hf * 8 + e] = __builtin_amdgcn_mfma_f32_16x16x16bf16_1k(pf, __builtin_bit_cast(s16x4, bb), acc[hf * 8 + e], 0, 0, 0);
        }
      MEMFENCE(); SCHED();
    };
    if (w == 4) {
      for (u32 tp = 8; tp < NPAIR; ++tp) {
#pragma unroll
        for (int d = 0; d < 2; ++d) {
          const u32 so = toff(2 * tp + d);
          const f32x4 sc = qk(d);
          loadK(d, so); pv(d, sc); loadV(d, so);
        }
      }
    }
#pragma unroll
    for (int d = 0; d < 2; ++d) { const f32x4 sc = qk(d); pv(d, sc); }
    l = x32_sum(x16_sum(l));
    dmark = (u32)(__builtin_amdgcn_s_memrealtime() - tstart);
#pragma unroll
    for (int e = 0; e < 8; ++e) { const float lo = (lg & 1) ? acc[e][1] : acc[e][0], hi = (lg & 1) ? acc[8 + e][1] : acc[8 + e][0]; o[e] = (lg >> 1) ? hi : lo; }
  }
  const int ridx = (lg & 1) * 256 + (lg >> 1) * 128 + lc * 8;
  if (w != 4) {
    float* red = (float*)(lds + L_RED) + ridx;
    float* ml = (float*)(lds + L_ML);
    *(f32x4*)(red + w * 512) = f32x4{o[0], o[1], o[2], o[3]}; *(f32x4*)(red + w * 512 + 4) = f32x4{o[4], o[5], o[6], o[7]};
    if (lane < 2) *(float2*)(ml + w * 4 + lane * 2) = make_float2(m, l);
    if (lds_arrive(C_PART) == 6) {
      const int h2 = (lg & 1) * 2;
      float M = NEG, L = 0.f;
      f32x4 a0 = {0.f, 0.f, 0.f, 0.f}, a1 = a0;
#pragma unroll
      for (int i = 0; i < 8; ++i) if (i != 4) M = fmaxf(M, ml[i * 4 + h2]);
#pragma unroll
      for (int i = 0; i < 8; ++i) if (i != 4) { const float e = ex2(ml[i * 4 + h2] - M);
        L = fmaf(ml[i * 4 + h2 + 1], e, L); a0 += *(const f32x4*)(red + i * 512) * e; a1 += *(const f32x4*)(red + i * 512 + 4) * e; }
      *(f32x4*)(red + 4 * 512) = a0; *(f32x4*)(red + 4 * 512 + 4) = a1;
      if (lc == 0 && lg < 2) *(float2*)(ml + 16 + h2) = make_float2(M, L);
      lds_arrive(C_MERGED);
    }
  } else {
    if (lane == 0) __hip_atomic_store(cnt + C_END, 1u, __ATOMIC_RELAXED, __HIP_MEMORY_SCOPE_WORKGROUP);
    lds_wait(C_MERGED, 1);
    const float* red = (const float*)(lds + L_RED) + 4 * 512 + ridx;
    const f32x4 ma = *(const f32x4*)((const float*)(lds + L_ML) + 16);
    const f32x4 a0 = *(const f32x4*)red, a1 = *(const f32x4*)(red + 4);
    const float m0 = dpp<0xA0>(m), m1 = dpp<0xF5>(m), l0 = dpp<0xA0>(l), l1 = dpp<0xF5>(l);
    const float mh = (lg & 1) ? m1 : m0, lh = (lg & 1) ? l1 : l0, Ma = (lg & 1) ? ma[2] : ma[0], La = (lg & 1) ? ma[3] : ma[1];
    const float M = fmaxf(mh, Ma), e0 = ex2(mh - M), e1 = ex2(Ma - M);
    const u32 rec = WS_RA + (g * 32 + s) * RA_BLK + ((lg & 1) * 16 + (lg >> 1) * 8 + (lc >> 1)) * 80;
    const u32x2 ep2 = *(const u32x2*)(lds + L_EP);
    u32x4 v0, v1;
#pragma unroll
    for (int e = 0; e < 4; ++e) { v0[e] = __float_as_uint(fmaf(o[e], e0, a0[e] * e1)); v1[e] = __float_as_uint(fmaf(o[4 + e], e0, a1[e] * e1)); }
    const u32 Mb = __float_as_uint(M), Lb = __float_as_uint(fmaf(lh, e0, La * e1));
    __builtin_amdgcn_raw_buffer_store_b128(v0, rW, rec + (lc & 1) * 32, 0, AUX_SC1);
    __builtin_amdgcn_raw_buffer_store_b128(v1, rW, rec + (lc & 1) * 32 + 16, 0, AUX_SC1);
    u32 ca = 0, cb = 0;
#pragma unroll
    for (int e = 0; e < 4; ++e) { chk(ca, cb, v0[e], (lc & 1) * 8 + e); chk(ca, cb, v1[e], (lc & 1) * 8 + 4 + e); }
    ca += dppu<XOR1>(ca); cb ^= dppu<XOR1>(cb);
    ca += ep2[0]; cb ^= ep2[1];
    chk(ca, cb, Mb, 16); chk(ca, cb, Lb, 17);
    __builtin_amdgcn_raw_buffer_store_b128(u32x4{Mb, Lb, ca, cb}, rW, (lc & 1) ? OOB : rec + 64, 0, AUX_SC1);
    MEMFENCE();
  }

  auto publish_c = [&](float y0, float y1) {
    const u32 b0 = __float_as_uint(y0), b1 = __float_as_uint(y1);
    u32 ca = 0, cb = 0;
    chk(ca, cb, b0, (lane & 7) * 2); chk(ca, cb, b1, (lane & 7) * 2 + 1);
    ca += dppu<XOR1>(ca); cb ^= dppu<XOR1>(cb); ca += dppu<XOR2>(ca); cb ^= dppu<XOR2>(cb); ca += dppu<XOR7>(ca); cb ^= dppu<XOR7>(cb);
    ca += (u32)epn; cb ^= (u32)(epn >> 32);
    const u32 z0 = dppu<XOR1>(b0), z1 = dppu<XOR1>(b1);
    const u32 rec = WS_RC + (w * 7 + (lane >> 3)) * RC_BLK + cslot * 128;
    const bool odd = lane & 1;
    const u32x4 ov = {odd ? ca : b0, odd ? cb : b1, odd ? ca : z0, odd ? cb : z1};
    __builtin_amdgcn_raw_buffer_store_b128(ov, rW, lane >= 56 ? OOB : !odd ? rec + (lane & 7) * 8 : (lane & 7) == 1 ? rec + 64 : OOB, 0, AUX_SC1);
    MEMFENCE();
  };
  auto gather_c = [&](int slot0, int nslot, u32 e0, u32 e1) {
    const u32 off = WS_RC + fid * RC_BLK + (slot0 + (lane >> 2)) * 128;
    u32x4 yv = {0u, 0u, 0u, 0u}; u32x2 cv;
    bool ok = (lane >> 2) >= nslot;
    for (u32 spins = 0; spins < SPIN_CAP; ++spins) {
      MEMFENCE();
      if (!ok) {
        yv = __builtin_amdgcn_raw_buffer_load_b128(rW, off + (lane & 3) * 16, 0, AUX_SC1);
        cv = __builtin_amdgcn_raw_buffer_load_b64(rW, off + 64, 0, AUX_SC1);
        u32 ca = 0, cb = 0;
#pragma unroll
        for (int e = 0; e < 4; ++e) chk(ca, cb, yv[e], (lane & 3) * 4 + e);
        ca += dppu<XOR1>(ca); cb ^= dppu<XOR1>(cb); ca += dppu<XOR2>(ca); cb ^= dppu<XOR2>(cb);
        u32 bad = ((ca + e0) ^ cv[0]) | ((cb ^ e1) ^ cv[1]);
        bad |= dppu<XOR1>(bad); bad |= dppu<XOR2>(bad);
        ok = bad == 0;
      }
      const int miss = __popcll(__ballot(!ok));
      if (miss == 0) break;
      for (int i = 8; i < miss; i += 8) __builtin_amdgcn_s_sleep(6);
    }
    const float v0 = t32_sum(__uint_as_float(yv[0]), __uint_as_float(yv[2])), v1 = t32_sum(__uint_as_float(yv[1]), __uint_as_float(yv[3]));
    float t = t16_sum(v0, v1);
    t += dpp<ROR4>(t); t += dpp<XOR8>(t);
    if (!(lane & 12)) ((float*)(lds + L_FIN))[(w ^ 4) * 16 + (lane & 3) * 4 + lg] = t;
  };

  u32 wp[64];
  if (w < 4 && (has_mlp || cons)) {
    const u32 rowoff = lane < 56 ? lane * 16 : OOB;
    const int l2 = w * 56 + (lane < 56 ? lane : 0);
    wait_until(T_CHORE + (((s - 16) & ~1) * 4 + (cons ? (s & 1) * 4 : 0) + w) * T_STAG);
    if (has_mlp) {
      const auto rW1 = __builtin_amdgcn_make_buffer_rsrc((void*)p.w1, 0, D * FF * 2, 0x00020000);
      const auto rW2 = __builtin_amdgcn_make_buffer_rsrc((void*)p.w2, 0, D * FF * 2, 0x00020000);
      const auto rB1 = __builtin_amdgcn_make_buffer_rsrc((void*)p.b1, 0, FF * 2, 0x00020000);
      const int u0 = mi * 32;
      u32x4 w1r[7], w2r[8];
#pragma unroll
      for (int i = 0; i < 7; ++i) w1r[i] = __builtin_amdgcn_raw_buffer_load_b128(rW1, (w * 16 + (lane >> 2)) * (FF * 2) + u0 * 2 + (lane & 3) * 16, i * 64 * (FF * 2), AUX_NT);
#pragma unroll
      for (int i = 0; i < 8; ++i) w2r[i] = __builtin_amdgcn_raw_buffer_load_b128(rW2, rowoff, (u0 + w * 8 + i) * 896, AUX_NT);
      const u32 b1r = __builtin_amdgcn_raw_buffer_load_b16(rB1, (u0 + w * 8 + (lane & 7)) * 2, 0, 0);
      gemv32(w1r);
      lds_arrive(C_HP);
      lds_wait(C_HP, 4);
      const float* hp = (const float*)(lds + L_QP) + w * 8 + (lane & 7);
      float hv = bfr(bfr((hp[0] + hp[32]) + (hp[64] + hp[96])) + bf2f_lo(b1r));
      hv = bfr(0.5f * hv * (1.f + tanhf(0.7978845608028654f * (hv + 0.044715f * hv * hv * hv))));
      float ym[8];
#pragma unroll
      for (int e = 0; e < 8; ++e) ym[e] = 0.f;
#pragma unroll
      for (int r = 0; r < 8; ++r) {
        const float hr = __builtin_bit_cast(float, __builtin_amdgcn_readlane(__builtin_bit_cast(int, hv), r));
#pragma unroll
        for (int e = 0; e < 4; ++e) { ym[2 * e] = fmaf(hr, bf2f_lo(w2r[r][e]), ym[2 * e]); ym[2 * e + 1] = fmaf(hr, bf2f_hi(w2r[r][e]), ym[2 * e + 1]); }
      }
      if (lane < 56) { float* y = (float*)(lds + L_YM) + w * 448 + lane * 8;
        *(f32x4*)y = f32x4{ym[0], ym[1], ym[2], ym[3]}; *(f32x4*)(y + 4) = f32x4{ym[4], ym[5], ym[6], ym[7]}; }
      lds_arrive(C_CHORE);
      lds_wait(C_CHORE, 4);
      const float2* y = (const float2*)(lds + L_YM) + l2;
      const float2 a = y[0], b = y[224], c = y[448], d = y[672];
      lds_wait(C_END, 1);
      publish_c((a.x + b.x) + (c.x + d.x), (a.y + b.y) + (c.y + d.y));
    } else {
      const auto rWo = __builtin_amdgcn_make_buffer_rsrc((void*)p.wo, 0, D * HQ * DH * 2, 0x00020000);
      u32x4 wor[16];
#pragma unroll
      for (int i = 0; i < 16; ++i) wor[i] = __builtin_amdgcn_raw_buffer_load_b128(rWo, rowoff, (g * 512 + (s - 24) * 64 + w * 16 + i) * 896, AUX_NT);
      if (lane < 56) {
#pragma unroll
        for (int i = 0; i < 16; ++i) *(u32x4*)(lds + L_WO + (w * 16 + i) * 896 + lane * 16) = wor[i];
      }
      lds_arrive(C_CHORE);
      if (fin && tid < 16) { const int col = fid * 16 + tid;
        ((float*)(lds + L_XB))[tid] = __bfloat162float(((const __hip_bfloat16*)p.x)[col]) + __bfloat162float(((const __hip_bfloat16*)p.b2)[col]); }
      lds_wait(C_CHORE, 4);
#pragma unroll
      for (int k = 0; k < 32; ++k) {
        const u32 a = ((const u32*)(lds + L_WO))[(2 * k) * 224 + l2], b = ((const u32*)(lds + L_WO))[(2 * k + 1) * 224 + l2];
        wp[2 * k] = __builtin_amdgcn_perm(b, a, 0x05040100u); wp[2 * k + 1] = __builtin_amdgcn_perm(b, a, 0x07060302u);
      }
    }
  }
  if (cons) {
    bar();
    const u32x2 ep2 = *(const u32x2*)(lds + L_EP);
    if (w >= 4) {
      const u32 roff = WS_RA + (g * 32 + (lane >> 2)) * RA_BLK + ((s - 24) * 4 + w - 4) * 80;
      u32x4 ov[2], mv[2];
      bool ok[2] = {false, false};
      for (u32 spins = 0; spins < SPIN_CAP; ++spins) {
        MEMFENCE();
#pragma unroll
        for (int i = 0; i < 2; ++i) if (!ok[i]) {
          ov[i] = __builtin_amdgcn_raw_buffer_load_b128(rW, roff + i * 16 * RA_BLK + (lane & 3) * 16, 0, AUX_SC1);
          mv[i] = __builtin_amdgcn_raw_buffer_load_b128(rW, roff + i * 16 * RA_BLK + 64, 0, AUX_SC1);
        }
        int miss = 0;
#pragma unroll
        for (int i = 0; i < 2; ++i) {
          u32 ca = 0, cb = 0;
#pragma unroll
          for (int e = 0; e < 4; ++e) chk(ca, cb, ov[i][e], (lane & 3) * 4 + e);
          ca += dppu<XOR1>(ca); cb ^= dppu<XOR1>(cb); ca += dppu<XOR2>(ca); cb ^= dppu<XOR2>(cb);
          ca += ep2[0]; cb ^= ep2[1];
          chk(ca, cb, mv[i][0], 16); chk(ca, cb, mv[i][1], 17);
          u32 bad = (ca ^ mv[i][2]) | (cb ^ mv[i][3]);
          bad |= dppu<XOR1>(bad); bad |= dppu<XOR2>(bad);
          ok[i] = bad == 0;
          miss += __popcll(__ballot(!ok[i]));
        }
        if (miss == 0) break;
        for (int i = 8; i < miss; i += 4) __builtin_amdgcn_s_sleep(2);
      }
      const float m0 = __uint_as_float(mv[0][0]), m1 = __uint_as_float(mv[1][0]);
      float M = fmaxf(m0, m1);
      M = fmaxf(M, dpp<ROR4>(M)); M = fmaxf(M, dpp<XOR8>(M)); M = x32_max(x16_max(M));
      const float w0 = ex2(m0 - M), w1 = ex2(m1 - M);
      float L = fmaf(w0, __uint_as_float(mv[0][1]), w1 * __uint_as_float(mv[1][1]));
      L += dpp<ROR4>(L); L += dpp<XOR8>(L); L = x32_sum(x16_sum(L));
      float oo[4];
#pragma unroll
      for (int e = 0; e < 4; ++e) oo[e] = fmaf(w0, __uint_as_float(ov[0][e]), w1 * __uint_as_float(ov[1][e]));
      oo[0] = t32_sum(oo[0], oo[2]); oo[1] = t32_sum(oo[1], oo[3]);
      float t = t16_sum(oo[0], oo[1]);
      t += dpp<ROR4>(t); t += dpp<XOR8>(t);
      if (!(lane & 12)) ((unsigned short*)(lds + L_O))[(w - 4) * 16 + (lane & 3) * 4 + lg] = (unsigned short)pk_bf16(t * __builtin_amdgcn_rcpf(L), 0.f);
    }
    bar();
    if (w < 4) {
      float y0 = 0.f, y1 = 0.f, y2 = 0.f, y3 = 0.f;
#pragma unroll
      for (int k4 = 0; k4 < 8; ++k4) { const u32x4 op = ((const u32x4*)(lds + L_O))[k4];
#pragma unroll
        for (int e = 0; e < 4; e += 2) {
          y0 = dot2(wp[8 * k4 + 2 * e], op[e], y0); y1 = dot2(wp[8 * k4 + 2 * e + 1], op[e], y1);
          y2 = dot2(wp[8 * k4 + 2 * e + 2], op[e + 1], y2); y3 = dot2(wp[8 * k4 + 2 * e + 3], op[e + 1], y3);
        } }
      publish_c(y0 + y2, y1 + y3);
    }
    if (fin) {
      gather_c((w ^ 4) * 16, w == 3 ? 8 : 16, ep2[0], ep2[1]);
      bar();
      if (tid < 16) {
        const float* f = (const float*)(lds + L_FIN) + tid;
        const float r = (((const float*)(lds + L_XB))[tid] + ((f[64] + f[80]) + (f[96] + f[112]))) + ((f[0] + f[16]) + (f[32] + f[48]));
        ((unsigned short*)p.out)[fid * 16 + tid] = (unsigned short)pk_bf16(r, 0.f);
        if (fid == 0 && tid == 0) __builtin_amdgcn_raw_buffer_store_b64(ep2, rW, WS_EPOCH, 0, AUX_SC1);
      }
    }
  }
  if (fin && fid == 0 && w == 4) { while ((u32)(__builtin_amdgcn_s_memrealtime() - tstart) < 20000 + (dmark - 6000) * 4) __builtin_amdgcn_s_sleep(4); }
}

extern "C" void kernel_launch(void* const* d_in, const int* in_sizes, int n_in, void* d_out, int out_size, void* d_ws, size_t ws_size, hipStream_t stream) {
  Params p;
  p.x = d_in[0]; p.k = d_in[1]; p.v = d_in[2]; p.wq = d_in[3]; p.wo = d_in[4];
  p.w1 = d_in[6]; p.b1 = d_in[7]; p.w2 = d_in[8]; p.b2 = d_in[9];
  p.out = d_out; p.ws = d_ws;
  hipLaunchKernelGGL(fused_decode, dim3(NWG), dim3(512), 0, stream, p);
}
```

```cpp
#include <hip/hip_runtime.h>
#include <hip/hip_bf16.h>
#include <stdint.h>

typedef unsigned u32;
typedef unsigned long long u64;
typedef u32 u32x4 __attribute__((ext_vector_type(4)));
typedef u32 u32x2 __attribute__((ext_vector_type(2)));
typedef float f32x4 __attribute__((ext_vector_type(4)));
typedef __bf16 bf16x8 __attribute__((ext_vector_type(8)));
typedef __bf16 bf16x2 __attribute__((ext_vector_type(2)));
typedef short s16x4 __attribute__((ext_vector_type(4)));
#define DI __device__ __forceinline__

constexpr u32 TMASK = 0x178, SPERM = 0x31420;
constexpr u32 T_QPOLL = 450, T_CHORE = 1200, T_STAG = 20;

constexpr int D = 448, HQ = 16, SK = 65536, DH = 256, FF = 1792;
constexpr int NWG = 256;
constexpr int NPAIR = 64;
constexpr int NFIN = 28;
constexpr u32 WS_EPOCH = 0;
constexpr u32 WS_QG = 4096;
constexpr u32 WS_RA = WS_QG + 8 * 512 * 8;
constexpr u32 RA_REC = 48;
constexpr u32 RA_BLK = 32 * RA_REC;
constexpr u32 WS_RC = WS_RA + NWG * RA_BLK;
constexpr u32 RC_BLK = 128 * 128;
constexpr u32 WS_END = WS_RC + NFIN * RC_BLK;
constexpr int AUX_NT = 2, AUX_SC1 = 16;
constexpr float NEG = -3.0e38f;
constexpr u32 SPIN_CAP = 1u << 15;
constexpr u32 OOB = 0x7fffff00u;

#define MEMFENCE() asm volatile("" ::: "memory")
#define SCHED() __builtin_amdgcn_sched_barrier(0)

DI float bf2f_lo(u32 x) { return __builtin_bit_cast(float, x << 16); }
DI float bf2f_hi(u32 x) { return __builtin_bit_cast(float, x & 0xffff0000u); }
typedef float f32x2 __attribute__((ext_vector_type(2)));
DI u32 pk_bf16(float a, float b) { const f32x2 f = {a, b}; return __builtin_bit_cast(u32, __builtin_convertvector(f, bf16x2)); }
DI float bfr(float a) { return bf2f_lo(pk_bf16(a, 0.f)); }
template <int CTRL> DI float dpp(float x) {
  return __builtin_bit_cast(float, __builtin_amdgcn_mov_dpp(__builtin_bit_cast(int, x), CTRL, 0xf, 0xf, true));
}
constexpr int XOR1 = 0xB1, XOR2 = 0x4E, XOR3 = 0x1B, XOR7 = 0x141, XOR8 = 0x128, ROR4 = 0x124;
DI float t32_sum(float u, float v) { auto s = __builtin_amdgcn_permlane32_swap(__float_as_uint(u), __float_as_uint(v), false, false); return __uint_as_float(s[0]) + __uint_as_float(s[1]); }
DI float t16_sum(float u, float v) { auto s = __builtin_amdgcn_permlane16_swap(__float_as_uint(u), __float_as_uint(v), false, false); return __uint_as_float(s[0]) + __uint_as_float(s[1]); }
DI float x16_sum(float x) { return t16_sum(x, x); }
DI float x32_sum(float x) { return t32_sum(x, x); }
DI float x16_max(float x) { auto s = __builtin_amdgcn_permlane16_swap(__float_as_uint(x), __float_as_uint(x), false, false); return fmaxf(__uint_as_float(s[0]), __uint_as_float(s[1])); }
DI float x32_max(float x) { auto s = __builtin_amdgcn_permlane32_swap(__float_as_uint(x), __float_as_uint(x), false, false); return fmaxf(__uint_as_float(s[0]), __uint_as_float(s[1])); }
DI float row_sum(float x) { x += dpp<XOR1>(x); x += dpp<XOR2>(x); x += dpp<XOR7>(x); x += dpp<XOR8>(x); return x; }
DI float wave_sum(float x) { return x32_sum(x16_sum(row_sum(x))); }
template <int CTRL> DI u32 dppu(u32 x) { return (u32)__builtin_amdgcn_mov_dpp((int)x, CTRL, 0xf, 0xf, true); }
DI u32 rotl(u32 x, u32 r) { return __builtin_amdgcn_alignbit(x, x, 32u - r); }
DI void chk(u32& ca, u32& cb, u32 v, u32 i) { ca += v; cb ^= rotl(v, i + 1); }
DI void bar() { asm volatile("s_waitcnt lgkmcnt(0)\n\ts_barrier" ::: "memory"); }
DI float dot2(u32 a, u32 b, float c) { return __builtin_amdgcn_fdot2_f32_bf16(__builtin_bit_cast(bf16x2, a), __builtin_bit_cast(bf16x2, b), c, false); }
DI float ex2(float x) { return __builtin_amdgcn_exp2f(x); }
template <u32 M> DI u32 pdep(u32 x) {
  u32 r = 0, k = 0;
#pragma unroll
  for (int b = 0; b < 12; ++b) if ((M >> b) & 1) { r |= ((x >> k) & 1u) << b; ++k; }
  return r;
}

struct Params {
  const void* x; const void* k; const void* v; const void* wq; const void* wo;
  const void* w1; const void* b1; const void* w2; const void* b2;
  void* out; void* ws;
};

constexpr int L_XN = 0;
constexpr int QSTR = 576;
constexpr int L_Q = L_XN + 2048;
constexpr int L_WO = L_Q + 2 * QSTR;
constexpr int L_QP = L_WO + 64 * 896;
constexpr int L_YM = L_QP + 512;
constexpr int L_RED = L_YM + 4 * 448 * 4;
constexpr int L_ML = L_RED + 8 * 2048;
constexpr int L_O = L_ML + 128;
constexpr int L_FIN = L_O + 128;
constexpr int L_XB = L_FIN + 512;
constexpr int L_EP = L_XB + 64;
constexpr int L_CNT = L_EP + 16;
constexpr int L_END = L_CNT + 64;
enum { C_QPART, C_QRDY, C_HP, C_CHORE, C_TURN, C_PART, C_MERGED, C_END };

__global__ __launch_bounds__(512, 2) void fused_decode(Params p) {
  __shared__ __attribute__((aligned(16))) char lds[L_END];
  const int tid = threadIdx.x, lane = tid & 63;
  const int w = __builtin_amdgcn_readfirstlane(tid >> 6);
  const int bid = blockIdx.x;
  const int g = ((bid >> 6) << 1) | ((bid >> 2) & 1);
  const int s = (((bid >> 4) & 3) << 3) | ((bid & 3) << 1) | ((bid >> 3) & 1);
  const int lg = lane >> 4, lc = lane & 15;
  if (w == 4) __builtin_amdgcn_s_setprio(3);
  const auto rW = __builtin_amdgcn_make_buffer_rsrc(p.ws, 0, WS_END, 0x00020000);
  u64 epn = 0;
  u32* const cnt = (u32*)(lds + L_CNT);
  float* const xn = (float*)(lds + L_XN);
  auto lds_arrive = [&](int i) -> u32 {
    asm volatile("s_waitcnt lgkmcnt(0)" ::: "memory");
    u32 r = 0;
    if (lane == 0) r = __hip_atomic_fetch_add(cnt + i, 1u, __ATOMIC_RELAXED, __HIP_MEMORY_SCOPE_WORKGROUP);
    r = __builtin_amdgcn_readfirstlane(r);
    MEMFENCE();
    return r;
  };
  auto lds_wait = [&](int i, u32 v) { while (__hip_atomic_load(cnt + i, __ATOMIC_RELAXED, __HIP_MEMORY_SCOPE_WORKGROUP) != v) __builtin_amdgcn_s_sleep(1); MEMFENCE(); };
  const u64 tstart = __builtin_amdgcn_s_memrealtime();
  auto wait_until = [&](u32 ticks) { while ((u32)(__builtin_amdgcn_s_memrealtime() - tstart) < ticks) __builtin_amdgcn_s_sleep(1); };
  const bool q_prod = s < 16, has_mlp = s >= 16 && s < (g < 4 ? 24 : 22), cons = s >= 24;
  const int mi = (g < 4 ? g * 8 : 8 + g * 6) + s - 16;
  const int cslot = cons ? (s - 24) * 8 + g : 64 + mi;
  const int fid = (s - 24) * 8 + g;
  const bool fin = cons && fid < NFIN;
  auto gemv32 = [&](const u32x4 (&wr)[7]) {
    float a[8];
#pragma unroll
    for (int e = 0; e < 8; ++e) a[e] = 0.f;
#pragma unroll
    for (int i = 0; i < 7; ++i) { const float xx = xn[16 * (w + 4 * i) + (lane >> 2)];
#pragma unroll
      for (int e = 0; e < 4; ++e) { a[2 * e] = fmaf(xx, bf2f_lo(wr[i][e]), a[2 * e]); a[2 * e + 1] = fmaf(xx, bf2f_hi(wr[i][e]), a[2 * e + 1]); } }
#pragma unroll
    for (int e = 0; e < 4; ++e) a[e] = t32_sum(a[e], a[e + 4]);
#pragma unroll
    for (int e = 0; e < 2; ++e) a[e] = t16_sum(a[e], a[e + 2]);
    const float t1 = a[0] + dpp<XOR8>(a[0]), t2 = a[1] + dpp<XOR8>(a[1]);
    float t = (lane & 8) ? t2 : t1;
    t += dpp<XOR7>(dpp<XOR3>(t));
    if (!(lane & 4)) ((float*)(lds + L_QP))[w * 32 + (lane & 3) * 8 + (lane >> 3)] = t;
  };
  u32x4 xr, wq[7];
  u32x2 ep = {0u, 0u};
  if (w < 4 || w == 5) ep = __builtin_amdgcn_raw_buffer_load_b64(rW, WS_EPOCH, 0, AUX_SC1);
  u32 tag_lo = 0, tag_hi = 0;
  auto take_epoch = [&]() {
    asm volatile("" : "+v"(ep));
    const u64 ep64 = ((u64)(u32)__builtin_amdgcn_readfirstlane(ep[1]) << 32) | (u32)__builtin_amdgcn_readfirstlane(ep[0]);
    epn = ep64 * 6364136223846793005ull + 1442695040888963407ull;
    tag_lo = (u32)epn << 16; tag_hi = (u32)(epn >> 16);
  };
  if (w < 4) {
    __builtin_amdgcn_s_setprio(2);
    const auto rX = __builtin_amdgcn_make_buffer_rsrc((void*)p.x, 0, (q_prod || has_mlp) ? D * 2 : 0, 0x00020000);
    const auto rWq = __builtin_amdgcn_make_buffer_rsrc((void*)p.wq, 0, q_prod ? D * HQ * DH * 2 : 0, 0x00020000);
    xr = __builtin_amdgcn_raw_buffer_load_b128(rX, lane * 16, 0, 0);
    const u32 wqo = (w * 16 + (lane >> 2)) * 8192 + (g * 512 + s * 32) * 2 + (lane & 3) * 16;
#pragma unroll
    for (int i = 0; i < 7; ++i) wq[i] = __builtin_amdgcn_raw_buffer_load_b128(rWq, wqo, i * 64 * 8192, AUX_NT);
  }
  if (w != 4) {
    if (tid < 16) cnt[tid] = 0;
    bar();
  }
  if (__builtin_expect(w < 4, 0)) {
    {
      float xv[8];
#pragma unroll
      for (int e = 0; e < 4; ++e) { xv[2 * e] = bf2f_lo(xr[e]); xv[2 * e + 1] = bf2f_hi(xr[e]); }
      const float mean = wave_sum(((xv[0] + xv[1]) + (xv[2] + xv[3])) + ((xv[4] + xv[5]) + (xv[6] + xv[7]))) * (1.f / D);
      float sq = 0.f;
#pragma unroll
      for (int e = 0; e < 8; ++e) { const float d = xv[e] - mean; sq = fmaf(d, d, sq); }
      sq = lane < 56 ? sq : 0.f;
      const float var = wave_sum(sq) * (1.f / D);
      const float mb = bfr(mean), den = bfr(__builtin_amdgcn_sqrtf(bfr(bfr(var) + 1e-5f)));
      const float rden = lane < 56 ? __builtin_amdgcn_rcpf(den) : 0.f;
      f32x4 o[2];
#pragma unroll
      for (int e = 0; e < 8; ++e) o[e >> 2][e & 3] = bfr(bfr(xv[e] - mb) * rden);
      *(f32x4*)(xn + lane * 8) = o[0]; *(f32x4*)(xn + lane * 8 + 4) = o[1];
    }
    take_epoch();
    if (q_prod) {
      gemv32(wq);
      if (lds_arrive(C_QPART) == 3) {
        const float* qp = (const float*)(lds + L_QP) + (lane & 31);
        const float q = (qp[0] + qp[32]) + (qp[64] + qp[96]);
        const u32x2 gr = {tag_lo | (pk_bf16(q, 0.f) & 0xffffu), tag_hi};
        __builtin_amdgcn_raw_buffer_store_b64(gr, rW, lane < 32 ? WS_QG + (g * 512 + s * 32 + lane) * 8 : OOB, 0, AUX_SC1);
      }
    }
    MEMFENCE();
    __builtin_amdgcn_s_setprio(0);
  }

  float m = NEG, l = 0.f, o[8];
  {
    const auto rK = __builtin_amdgcn_make_buffer_rsrc((void*)p.k, 0, 0x10000000, 0x00020000);
    const auto rV = __builtin_amdgcn_make_buffer_rsrc((void*)p.v, 0, 0x10000000, 0x00020000);
    u32x4 kb[2][8], vb[2][8];
    const u32 koff = (4 * (lc & 3) + (lc >> 2)) * 512 + lg * 16;
    const u32 voff = lg * 512 + lc * 16;
    u32 sp = 0;
#pragma unroll
    for (int i = 0; i < 5; ++i) sp |= ((s >> i) & 1u) << ((SPERM >> (4 * i)) & 7);
    const u32 tbase = g * (u32)(SK * 512) + (pdep<TMASK>(sp) << 13);
    auto toff = [&](u32 i) -> u32 { return tbase + (pdep<~TMASK & 0xfff>(i) << 13); };
    auto loadK = [&](int d, u32 so) {
      u32 oo = koff; asm volatile("" : "+v"(oo));
#pragma unroll
      for (int j = 0; j < 8; ++j) kb[d][j] = __builtin_amdgcn_raw_buffer_load_b128(rK, oo + j * 64, so, AUX_NT);
      MEMFENCE(); SCHED();
    };
    auto loadV = [&](int d, u32 so) {
      u32 oo = voff; asm volatile("" : "+v"(oo));
#pragma unroll
      for (int j = 0; j < 8; ++j) vb[d][j] = __builtin_amdgcn_raw_buffer_load_b128(rV, oo + ((j >> 1) & 1) * 2048 + (j & 1) * 256, so + (j >> 2) * 4096, AUX_NT);
      MEMFENCE(); SCHED();
    };
    const u32 pair0 = w ^ 4;
    {
      const u32 so0 = toff(2 * pair0), so1 = toff(2 * pair0 + 1);
      asm volatile("" :: "s"(so0), "s"(so1), "v"(koff), "v"(voff));
      if (pair0) lds_wait(C_TURN, pair0);
      loadK(0, so0);
      if (w == 4) { __builtin_amdgcn_s_setprio(0); bar(); }
      loadV(0, so0); loadK(1, so1); loadV(1, so1);
      if (lane == 0) __hip_atomic_store(cnt + C_TURN, pair0 + 1, __ATOMIC_RELAXED, __HIP_MEMORY_SCOPE_WORKGROUP);
      MEMFENCE();
    }
    if (w == 5) {
      take_epoch();
      {
        const u32 qgoff = WS_QG + (g * 512 + lane * 2) * 8;
        u32x4 q[4];
        bool ok[4] = {false, false, false, false};
        wait_until(T_QPOLL);
        for (u32 spins = 0; spins < SPIN_CAP; ++spins) {
          MEMFENCE();
#pragma unroll
          for (int i = 0; i < 4; ++i) if (!ok[i]) q[i] = __builtin_amdgcn_raw_buffer_load_b128(rW, qgoff + i * 1024, 0, AUX_SC1);
#pragma unroll
          for (int i = 0; i < 4; ++i) ok[i] = ((q[i][0] & 0xffff0000u) == tag_lo) & (q[i][1] == tag_hi) & ((q[i][2] & 0xffff0000u) == tag_lo) & (q[i][3] == tag_hi);
          if (__all(ok[0] & ok[1] & ok[2] & ok[3])) break;
        }
#pragma unroll
        for (int i = 0; i < 4; ++i) *(u32*)(lds + L_Q + (i >> 1) * QSTR + (i & 1) * 256 + lane * 4) = (q[i][0] & 0xffffu) | (q[i][2] << 16);
      }
      if (lane == 0) *(u64*)(lds + L_EP) = epn;
      lds_arrive(C_QRDY);
    }
    lds_wait(C_QRDY, 1);
    if (w == 4) lds_wait(C_TURN, 8);
    bf16x8 qf[8];
#pragma unroll
    for (int j = 0; j < 8; ++j) qf[j] = *(const bf16x8*)(lds + L_Q + (lc & 1) * QSTR + lg * 16 + j * 64);
    constexpr float C2 = 0.0625f * 1.4426950408889634f;
    f32x4 acc[16];
#pragma unroll
    for (int i = 0; i < 16; ++i) acc[i] = f32x4{0.f, 0.f, 0.f, 0.f};
    auto qk = [&](int d) -> f32x4 {
      f32x4 sc = {0.f, 0.f, 0.f, 0.f};
#pragma unroll
      for (int j = 0; j < 8; ++j) sc = __builtin_amdgcn_mfma_f32_16x16x32_bf16(__builtin_bit_cast(bf16x8, kb[d][j]), qf[j], sc, 0, 0, 0);
      asm volatile("" : "+v"(sc) :: "memory"); SCHED();
      return sc;
    };
    auto pv = [&](int d, f32x4 sc) {
      const u32 sa = pk_bf16(sc[0], sc[1]), sb = pk_bf16(sc[2], sc[3]);
      const float t0 = bf2f_lo(sa), t1 = bf2f_hi(sa), t2 = bf2f_lo(sb), t3 = bf2f_hi(sb);
      const float tl = fmaxf(fmaxf(t0, t1), fmaxf(t2, t3)) * C2;
      if (__any(tl > m)) {
        const float mn = fmaxf(m, x32_max(x16_max(tl))), al = ex2(m - mn);
        m = mn; l *= al;
        const float a0 = dpp<0xA0>(al), a1 = dpp<0xF5>(al);
#pragma unroll
        for (int i = 0; i < 16; ++i) { acc[i][0] *= a0; acc[i][1] *= a1; }
      }
      const float p0 = ex2(fmaf(t0, C2, -m)), p1 = ex2(fmaf(t1, C2, -m)), p2 = ex2(fmaf(t2, C2, -m)), p3 = ex2(fmaf(t3, C2, -m));
      l += (p0 + p1) + (p2 + p3);
      const u32x2 pp = {pk_bf16(p0, p1), pk_bf16(p2, p3)};
      const s16x4 pf = __builtin_bit_cast(s16x4, pp);
#pragma unroll
      for (int hf = 0; hf < 2; ++hf)
#pragma unroll
        for (int e = 0; e < 8; ++e) {
          const u32 sel = (e & 1) ? 0x07060302u : 0x05040100u;
          const u32x2 bb = {__builtin_amdgcn_perm(vb[d][2 + hf][e >> 1], vb[d][hf][e >> 1], sel), __builtin_amdgcn_perm(vb[d][6 + hf][e >> 1], vb[d][4 + hf][e >> 1], sel)};
          acc[hf * 8 + e] = __builtin_amdgcn_mfma_f32_16x16x16bf16_1k(pf, __builtin_bit_cast(s16x4, bb), acc[hf * 8 + e], 0, 0, 0);
        }
      MEMFENCE(); SCHED();
    };
    if (w == 4) {
      for (u32 tp = 8; tp < NPAIR; ++tp) {
#pragma unroll
        for (int d = 0; d < 2; ++d) {
          const u32 so = toff(2 * tp + d);
          const f32x4 sc = qk(d);
          loadK(d, so); pv(d, sc); loadV(d, so);
        }
      }
    }
#pragma unroll
    for (int d = 0; d < 2; ++d) { const f32x4 sc = qk(d); pv(d, sc); }
    l = x32_sum(x16_sum(l));
#pragma unroll
    for (int e = 0; e < 8; ++e) { const float lo = (lg & 1) ? acc[e][1] : acc[e][0], hi = (lg & 1) ? acc[8 + e][1] : acc[8 + e][0]; o[e] = (lg >> 1) ? hi : lo; }
  }
  const int ridx = (lg & 1) * 256 + (lg >> 1) * 128 + lc * 8;
  if (w != 4) {
    float* red = (float*)(lds + L_RED) + ridx;
    float* ml = (float*)(lds + L_ML);
    *(f32x4*)(red + w * 512) = f32x4{o[0], o[1], o[2], o[3]}; *(f32x4*)(red + w * 512 + 4) = f32x4{o[4], o[5], o[6], o[7]};
    if (lane < 2) *(float2*)(ml + w * 4 + lane * 2) = make_float2(m, l);
    if (lds_arrive(C_PART) == 6) {
      const int h2 = (lg & 1) * 2;
      float M = NEG, L = 0.f;
      f32x4 a0 = {0.f, 0.f, 0.f, 0.f}, a1 = a0;
#pragma unroll
      for (int i = 0; i < 8; ++i) if (i != 4) M = fmaxf(M, ml[i * 4 + h2]);
#pragma unroll
      for (int i = 0; i < 8; ++i) if (i != 4) { const float e = ex2(ml[i * 4 + h2] - M);
        L = fmaf(ml[i * 4 + h2 + 1], e, L); a0 += *(const f32x4*)(red + i * 512) * e; a1 += *(const f32x4*)(red + i * 512 + 4) * e; }
      *(f32x4*)(red + 4 * 512) = a0; *(f32x4*)(red + 4 * 512 + 4) = a1;
      if (lc == 0 && lg < 2) *(float2*)(ml + 16 + h2) = make_float2(M, L);
      lds_arrive(C_MERGED);
    }
  } else {
    if (lane == 0) __hip_atomic_store(cnt + C_END, 1u, __ATOMIC_RELAXED, __HIP_MEMORY_SCOPE_WORKGROUP);
    lds_wait(C_MERGED, 1);
    const float* red = (const float*)(lds + L_RED) + 4 * 512 + ridx;
    const f32x4 ma = *(const f32x4*)((const float*)(lds + L_ML) + 16);
    const f32x4 a0 = *(const f32x4*)red, a1 = *(const f32x4*)(red + 4);
    const float m0 = dpp<0xA0>(m), m1 = dpp<0xF5>(m), l0 = dpp<0xA0>(l), l1 = dpp<0xF5>(l);
    const float mh = (lg & 1) ? m1 : m0, lh = (lg & 1) ? l1 : l0, Ma = (lg & 1) ? ma[2] : ma[0], La = (lg & 1) ? ma[3] : ma[1];
    const float M = fmaxf(mh, Ma), e0 = ex2(mh - M), e1 = ex2(Ma - M);
    const u32x2 ep2 = *(const u32x2*)(lds + L_EP);
    const u32x4 vp = {pk_bf16(fmaf(o[0], e0, a0[0] * e1), fmaf(o[1], e0, a0[1] * e1)), pk_bf16(fmaf(o[2], e0, a0[2] * e1), fmaf(o[3], e0, a0[3] * e1)),
                      pk_bf16(fmaf(o[4], e0, a1[0] * e1), fmaf(o[5], e0, a1[1] * e1)), pk_bf16(fmaf(o[6], e0, a1[2] * e1), fmaf(o[7], e0, a1[3] * e1))};
    const u32 Mb = __float_as_uint(M), Lb = __float_as_uint(fmaf(lh, e0, La * e1));
    u32 ca = 0, cb = 0;
#pragma unroll
    for (int e = 0; e < 4; ++e) chk(ca, cb, vp[e], (lc & 1) * 4 + e);
    ca += dppu<XOR1>(ca); cb ^= dppu<XOR1>(cb);
    ca += ep2[0]; cb ^= ep2[1];
    chk(ca, cb, Mb, 16); chk(ca, cb, Lb, 17);
    char* stg = lds + L_RED;
    const u32 ro = ((lg & 1) * 16 + (lg >> 1) * 8 + (lc >> 1)) * RA_REC;
    *(u32x4*)(stg + ro + (lc & 1) * 16) = vp;
    if (!(lc & 1)) *(u32x4*)(stg + ro + 32) = u32x4{Mb, Lb, ca, cb};
    asm volatile("s_waitcnt lgkmcnt(0)" ::: "memory");
    const u32x4 s0 = *(const u32x4*)(stg + lane * 16), s1 = *(const u32x4*)(stg + 1024 + lane * 16);
    const u32 blk = WS_RA + (g * 32 + s) * RA_BLK + lane * 16;
    __builtin_amdgcn_raw_buffer_store_b128(s0, rW, blk, 0, AUX_SC1);
    __builtin_amdgcn_raw_buffer_store_b128(s1, rW, lane < 32 ? blk + 1024 : OOB, 0, AUX_SC1);
    MEMFENCE();
  }

  auto publish_c = [&](f32x4 y) {
    const u32x4 yb = __builtin_bit_cast(u32x4, y);
    u32 ca = 0, cb = 0;
#pragma unroll
    for (int e = 0; e < 4; ++e) chk(ca, cb, yb[e], (lane & 3) * 4 + e);
    ca += dppu<XOR1>(ca); cb ^= dppu<XOR1>(cb); ca += dppu<XOR2>(ca); cb ^= dppu<XOR2>(cb);
    ca += (u32)epn; cb ^= (u32)(epn >> 32);
    const u32x4 ov = (lane & 4) ? u32x4{ca, cb, ca, cb} : yb;
    __builtin_amdgcn_raw_buffer_store_b128(ov, rW, lane < 56 ? WS_RC + (w * 7 + (lane >> 3)) * RC_BLK + cslot * 128 + (lane & 7) * 16 : OOB, 0, AUX_SC1);
    MEMFENCE();
  };
  auto gather_c = [&](int slot0, int nslot, u32 e0, u32 e1) {
    const u32 off = WS_RC + fid * RC_BLK + (slot0 + (lane >> 2)) * 128;
    u32x4 yv = {0u, 0u, 0u, 0u}; u32x2 cv;
    bool ok = (lane >> 2) >= nslot;
    for (u32 spins = 0; spins < SPIN_CAP; ++spins) {
      MEMFENCE();
      if (!ok) {
        yv = __builtin_amdgcn_raw_buffer_load_b128(rW, off + (lane & 3) * 16, 0, AUX_SC1);
        cv = __builtin_amdgcn_raw_buffer_load_b64(rW, off + 64, 0, AUX_SC1);
        u32 ca = 0, cb = 0;
#pragma unroll
        for (int e = 0; e < 4; ++e) chk(ca, cb, yv[e], (lane & 3) * 4 + e);
        ca += dppu<XOR1>(ca); cb ^= dppu<XOR1>(cb); ca += dppu<XOR2>(ca); cb ^= dppu<XOR2>(cb);
        u32 bad = ((ca + e0) ^ cv[0]) | ((cb ^ e1) ^ cv[1]);
        bad |= dppu<XOR1>(bad); bad |= dppu<XOR2>(bad);
        ok = bad == 0;
      }
      const int miss = __popcll(__ballot(!ok));
      if (miss == 0) break;
      for (int i = 8; i < miss; i += 8) __builtin_amdgcn_s_sleep(6);
    }
    const float v0 = t32_sum(__uint_as_float(yv[0]), __uint_as_float(yv[2])), v1 = t32_sum(__uint_as_float(yv[1]), __uint_as_float(yv[3]));
    float t = t16_sum(v0, v1);
    t += dpp<ROR4>(t); t += dpp<XOR8>(t);
    if (!(lane & 12)) ((float*)(lds + L_FIN))[(w ^ 4) * 16 + (lane & 3) * 4 + lg] = t;
  };

  u32 wp[64];
  if (w < 4 && (has_mlp || cons)) {
    const u32 rowoff = lane < 56 ? lane * 16 : OOB;
    const int c4 = w * 28 + (lane < 56 ? (lane >> 3) * 4 + (lane & 3) : 0);
    wait_until(T_CHORE + (((s - 16) & ~1) * 4 + (cons ? (s & 1) * 4 : 0) + w) * T_STAG);
    if (has_mlp) {
      const auto rW1 = __builtin_amdgcn_make_buffer_rsrc((void*)p.w1, 0, D * FF * 2, 0x00020000);
      const auto rW2 = __builtin_amdgcn_make_buffer_rsrc((void*)p.w2, 0, D * FF * 2, 0x00020000);
      const auto rB1 = __builtin_amdgcn_make_buffer_rsrc((void*)p.b1, 0, FF * 2, 0x00020000);
      const int u0 = mi * 32;
      u32x4 w1r[7], w2r[8];
#pragma unroll
      for (int i = 0; i < 7; ++i) w1r[i] = __builtin_amdgcn_raw_buffer_load_b128(rW1, (w * 16 + (lane >> 2)) * (FF * 2) + u0 * 2 + (lane & 3) * 16, i * 64 * (FF * 2), AUX_NT);
#pragma unroll
      for (int i = 0; i < 8; ++i) w2r[i] = __builtin_amdgcn_raw_buffer_load_b128(rW2, rowoff, (u0 + w * 8 + i) * 896, AUX_NT);
      const u32 b1r = __builtin_amdgcn_raw_buffer_load_b16(rB1, (u0 + w * 8 + (lane & 7)) * 2, 0, 0);
      gemv32(w1r);
      lds_arrive(C_HP);
      lds_wait(C_HP, 4);
      const float* hp = (const float*)(lds + L_QP) + w * 8 + (lane & 7);
      float hv = bfr(bfr((hp[0] + hp[32]) + (hp[64] + hp[96])) + bf2f_lo(b1r));
      hv = bfr(0.5f * hv * (1.f + tanhf(0.7978845608028654f * (hv + 0.044715f * hv * hv * hv))));
      float ym[8];
#pragma unroll
      for (int e = 0; e < 8; ++e) ym[e] = 0.f;
#pragma unroll
      for (int r = 0; r < 8; ++r) {
        const float hr = __builtin_bit_cast(float, __builtin_amdgcn_readlane(__builtin_bit_cast(int, hv), r));
#pragma unroll
        for (int e = 0; e < 4; ++e) { ym[2 * e] = fmaf(hr, bf2f_lo(w2r[r][e]), ym[2 * e]); ym[2 * e + 1] = fmaf(hr, bf2f_hi(w2r[r][e]), ym[2 * e + 1]); }
      }
      if (lane < 56) { float* y = (float*)(lds + L_YM) + w * 448 + lane * 8;
        *(f32x4*)y = f32x4{ym[0], ym[1], ym[2], ym[3]}; *(f32x4*)(y + 4) = f32x4{ym[4], ym[5], ym[6], ym[7]}; }
      lds_arrive(C_CHORE);
      lds_wait(C_CHORE, 4);
      const f32x4* y = (const f32x4*)(lds + L_YM) + c4;
      const f32x4 ys = (y[0] + y[112]) + (y[224] + y[336]);
      lds_wait(C_END, 1);
      publish_c(ys);
    } else {
      const auto rWo = __builtin_amdgcn_make_buffer_rsrc((void*)p.wo, 0, D * HQ * DH * 2, 0x00020000);
      u32x4 wor[16];
#pragma unroll
      for (int i = 0; i < 16; ++i) wor[i] = __builtin_amdgcn_raw_buffer_load_b128(rWo, rowoff, (g * 512 + (s - 24) * 64 + w * 16 + i) * 896, AUX_NT);
      if (lane < 56) {
#pragma unroll
        for (int i = 0; i < 16; ++i) *(u32x4*)(lds + L_WO + (w * 16 + i) * 896 + lane * 16) = wor[i];
      }
      lds_arrive(C_CHORE);
      if (fin && tid < 16) { const int col = fid * 16 + tid;
        ((float*)(lds + L_XB))[tid] = __bfloat162float(((const __hip_bfloat16*)p.x)[col]) + __bfloat162float(((const __hip_bfloat16*)p.b2)[col]); }
      lds_wait(C_CHORE, 4);
#pragma unroll
      for (int k = 0; k < 16; ++k) {
        const u32x2* wl = (const u32x2*)(lds + L_WO) + ((lane & 4) * 8 + 2 * k) * 112 + c4;
        const u32x2 a = wl[0], b = wl[112];
#pragma unroll
        for (int e = 0; e < 4; ++e) wp[4 * k + e] = __builtin_amdgcn_perm(b[e >> 1], a[e >> 1], (e & 1) ? 0x07060302u : 0x05040100u);
      }
    }
  }
  if (cons) {
    bar();
    const u32x2 ep2 = *(const u32x2*)(lds + L_EP);
    if (w >= 4) {
      const u32 roff = WS_RA + (g * 32 + (lane >> 2)) * RA_BLK + ((s - 24) * 4 + w - 4) * RA_REC;
      u32x2 ov[2]; u32x4 mv[2];
      bool ok[2] = {false, false};
      for (u32 spins = 0; spins < SPIN_CAP; ++spins) {
        MEMFENCE();
#pragma unroll
        for (int i = 0; i < 2; ++i) if (!ok[i]) {
          ov[i] = __builtin_amdgcn_raw_buffer_load_b64(rW, roff + i * 16 * RA_BLK + (lane & 3) * 8, 0, AUX_SC1);
          mv[i] = __builtin_amdgcn_raw_buffer_load_b128(rW, roff + i * 16 * RA_BLK + 32, 0, AUX_SC1);
        }
        int miss = 0;
#pragma unroll
        for (int i = 0; i < 2; ++i) {
          u32 ca = 0, cb = 0;
          chk(ca, cb, ov[i][0], (lane & 3) * 2); chk(ca, cb, ov[i][1], (lane & 3) * 2 + 1);
          ca += dppu<XOR1>(ca); cb ^= dppu<XOR1>(cb); ca += dppu<XOR2>(ca); cb ^= dppu<XOR2>(cb);
          ca += ep2[0]; cb ^= ep2[1];
          chk(ca, cb, mv[i][0], 16); chk(ca, cb, mv[i][1], 17);
          u32 bad = (ca ^ mv[i][2]) | (cb ^ mv[i][3]);
          bad |= dppu<XOR1>(bad); bad |= dppu<XOR2>(bad);
          ok[i] = bad == 0;
          miss += __popcll(__ballot(!ok[i]));
        }
        if (miss == 0) break;
        for (int i = 8; i < miss; i += 4) __builtin_amdgcn_s_sleep(2);
      }
      const float m0 = __uint_as_float(mv[0][0]), m1 = __uint_as_float(mv[1][0]);
      float M = fmaxf(m0, m1);
      M = fmaxf(M, dpp<ROR4>(M)); M = fmaxf(M, dpp<XOR8>(M)); M = x32_max(x16_max(M));
      const float w0 = ex2(m0 - M), w1 = ex2(m1 - M);
      float L = fmaf(w0, __uint_as_float(mv[0][1]), w1 * __uint_as_float(mv[1][1]));
      L += dpp<ROR4>(L); L += dpp<XOR8>(L); L = x32_sum(x16_sum(L));
      float oo[4];
#pragma unroll
      for (int e = 0; e < 2; ++e) { oo[2 * e] = fmaf(w0, bf2f_lo(ov[0][e]), w1 * bf2f_lo(ov[1][e])); oo[2 * e + 1] = fmaf(w0, bf2f_hi(ov[0][e]), w1 * bf2f_hi(ov[1][e])); }
      oo[0] = t32_sum(oo[0], oo[2]); oo[1] = t32_sum(oo[1], oo[3]);
      float t = t16_sum(oo[0], oo[1]);
      t += dpp<ROR4>(t); t += dpp<XOR8>(t);
      if (!(lane & 12)) ((unsigned short*)(lds + L_O))[(w - 4) * 16 + (lane & 3) * 4 + lg] = (unsigned short)pk_bf16(t * __builtin_amdgcn_rcpf(L), 0.f);
    }
    bar();
    if (w < 4) {
      f32x4 y = {0.f, 0.f, 0.f, 0.f};
#pragma unroll
      for (int k4 = 0; k4 < 4; ++k4) { const u32x4 op = ((const u32x4*)(lds + L_O))[(lane & 4) + k4];
#pragma unroll
        for (int k = 0; k < 4; ++k)
#pragma unroll
          for (int e = 0; e < 4; ++e) y[e] = dot2(wp[16 * k4 + 4 * k + e], op[k], y[e]); }
#pragma unroll
      for (int e = 0; e < 4; ++e) { const float o4 = dpp<XOR7>(dpp<XOR3>(y[e])); y[e] = (lane & 4) ? o4 + y[e] : y[e] + o4; }
      publish_c(y);
    }
    if (fin) {
      gather_c((w ^ 4) * 16, w == 3 ? 8 : 16, ep2[0], ep2[1]);
      bar();
      if (tid < 16) {
        const float* f = (const float*)(lds + L_FIN) + tid;
        const float r = (((const float*)(lds + L_XB))[tid] + ((f[64] + f[80]) + (f[96] + f[112]))) + ((f[0] + f[16]) + (f[32] + f[48]));
        ((unsigned short*)p.out)[fid * 16 + tid] = (unsigned short)pk_bf16(r, 0.f);
        if (fid == 0 && tid == 0) __builtin_amdgcn_raw_buffer_store_b64(ep2, rW, WS_EPOCH, 0, AUX_SC1);
      }
    }
  }
}

extern "C" void kernel_launch(void* const* d_in, const int* in_sizes, int n_in, void* d_out, int out_size, void* d_ws, size_t ws_size, hipStream_t stream) {
  Params p;
  p.x = d_in[0]; p.k = d_in[1]; p.v = d_in[2]; p.wq = d_in[3]; p.wo = d_in[4];
  p.w1 = d_in[6]; p.b1 = d_in[7]; p.w2 = d_in[8]; p.b2 = d_in[9];
  p.out = d_out; p.ws = d_ws;
  hipLaunchKernelGGL(fused_decode, dim3(NWG), dim3(512), 0, stream, p);
}
```
